```python
import torch

def _softmax(x, axis=-1):
    return torch.softmax(x, dim=axis)
def _gelu(x):
    return torch.nn.functional.gelu(x, approximate="tanh")
def _silu(x):
    return torch.nn.functional.silu(x)
def _sigmoid(x):
    return torch.sigmoid(x)
def _layernorm(x, axis=-1, eps=1e-5):
    m = x.mean(dim=axis, keepdim=True)
    v = x.var(dim=axis, keepdim=True, unbiased=False)
    return (x - m) / torch.sqrt(v + eps)
def _scatter_add(tgt, idx, src, axis=0):
    return tgt.clone().index_add_(axis, idx, src)
def _segment_sum(src, seg, n):
    out = torch.zeros((n,) + src.shape[1:], dtype=src.dtype)
    return out.index_add_(0, seg, src)
def _pad(x, pw):
    flat = tuple(v for pair in reversed(pw) for v in pair)
    return torch.nn.functional.pad(x, flat)

kernel_name = 'standalone__gqa_block_cached__near_ridge'

def setup_inputs():

    _w = (lambda a: (lambda t: t.to(torch.bfloat16) if t.is_floating_point() else t)(torch.as_tensor(a)))
    B, Sq, D = 8, 64, 512
    Hq, Hkv, Sk, Dh = 32, 8, 512, 128
    assert Hq % Hkv == 0, (Hq, Hkv)
    return {
        "x": _w((lambda *s: torch.randn(*s, dtype=torch.bfloat16) * 0.8)(B, Sq, D)),
        "k_cache": _w((lambda *s: torch.randn(*s, dtype=torch.bfloat16) * 0.8)(B, Hkv, Sk, Dh)),
        "v_cache": _w((lambda *s: torch.randn(*s, dtype=torch.bfloat16) * 0.05)(B, Hkv, Sk, Dh)),
        "wq": _w((lambda *s: torch.randn(*s, dtype=torch.bfloat16) * 0.8)(D, Hq * Dh) * (D ** -0.5)),
        "wo": _w((lambda *s: torch.randn(*s, dtype=torch.bfloat16) * 0.05)(Hq * Dh, D) * ((Hq * Dh) ** -0.5)),
    }

def reference(x, k_cache, v_cache, wq, wo):
    B, Sq, D = x.shape
    Hq, Dh = 32, 128
    Hkv = k_cache.shape[1]
    rep = Hq // Hkv
    q = (lambda x, *d: x.permute(*d))((x @ wq).reshape(B, Sq, Hq, Dh), 0, 2, 1, 3)
    k = (lambda x, n, axis: torch.repeat_interleave(x, n, dim=axis))(k_cache, rep, axis=1)
    v = (lambda x, n, axis: torch.repeat_interleave(x, n, dim=axis))(v_cache, rep, axis=1)
    scores = torch.einsum("bhqd,bhkd->bhqk", q, k) * (Dh ** -0.5)
    p = _softmax(scores, axis=-1)
    o = torch.einsum("bhqk,bhkd->bhqd", p, v)
    return (lambda x, *d: x.permute(*d))(o, 0, 2, 1, 3).reshape(B, Sq, Hq * Dh) @ wo
```

```cpp
#include <hip/hip_runtime.h>
#include <hip/hip_bf16.h>

typedef unsigned short u16;
typedef __bf16 bf16x8 __attribute__((ext_vector_type(8)));
typedef __bf16 bf16x4 __attribute__((ext_vector_type(4)));
typedef __bf16 bf16x2 __attribute__((ext_vector_type(2)));
typedef short s16x4 __attribute__((ext_vector_type(4)));
typedef float f32x16 __attribute__((ext_vector_type(16)));
typedef float f32x4 __attribute__((ext_vector_type(4)));
typedef float f32x2 __attribute__((ext_vector_type(2)));
typedef unsigned u32x4 __attribute__((ext_vector_type(4)));
typedef unsigned u32x2 __attribute__((ext_vector_type(2)));
#define LAS __attribute__((address_space(3)))
#define DI __device__ __forceinline__
#define MFMA32(a, b, c) __builtin_amdgcn_mfma_f32_32x32x16_bf16((a), (b), (c), 0, 0, 0)

DI unsigned pk2(float a, float b) { f32x2 v = {a, b}; bf16x2 r = __builtin_convertvector(v, bf16x2); return __builtin_bit_cast(unsigned, r); }
DI bf16x8 pack8(const f32x16& x, int s) {
  u32x4 p = {pk2(x[8 * s], x[8 * s + 1]), pk2(x[8 * s + 2], x[8 * s + 3]), pk2(x[8 * s + 4], x[8 * s + 5]), pk2(x[8 * s + 6], x[8 * s + 7])};
  return __builtin_bit_cast(bf16x8, p);
}
DI bf16x8 trread2(const char* lds_base, unsigned a0, unsigned a1) {
  s16x4 lo = __builtin_amdgcn_ds_read_tr16_b64_v4i16((LAS s16x4*)(lds_base + a0));
  s16x4 hi = __builtin_amdgcn_ds_read_tr16_b64_v4i16((LAS s16x4*)(lds_base + a1));
  return __builtin_bit_cast(bf16x8, __builtin_shufflevector(lo, hi, 0, 1, 2, 3, 4, 5, 6, 7));
}
DI void glds16(const void* sbase, unsigned voff, unsigned lds_dst) {
  unsigned keep;
  asm volatile("s_mov_b32 %0, m0\n\ts_mov_b32 m0, %3\n\ts_nop 0\n\tglobal_load_lds_dwordx4 %1, %2\n\ts_mov_b32 m0, %0"
               : "=&s"(keep) : "v"(voff), "s"(sbase), "s"(lds_dst) : "memory");
}
template <int N> DI void wait_vm() { asm volatile("s_waitcnt vmcnt(%0)" ::"n"(N) : "memory"); }
DI void bar() { asm volatile("s_waitcnt lgkmcnt(0)" ::: "memory"); __builtin_amdgcn_s_barrier(); asm volatile("" ::: "memory"); }

constexpr int UNIT = 16384, SLOTS = 7, NUNITS = 28;
constexpr int QIMG = SLOTS * UNIT;
constexpr int OEX = QIMG + UNIT;
constexpr int LDS1 = OEX + 2 * UNIT;

constexpr int first_needed(int s) { return s < 12 ? (s / 3) * 3 : s; }
constexpr int issued_upto(int s) {
  int lim = (s < 0 ? 0 : first_needed(s)) + SLOTS; return lim < NUNITS ? lim : NUNITS;
}
constexpr int prev_stage(int s) {
  int p = s - 1; if (p >= 0 && p < 12 && p % 3 == 0) p -= 1; return p;
}

__global__ __launch_bounds__(256, 1) void attn_kernel(const u16* __restrict__ x, const u16* __restrict__ kc, const u16* __restrict__ vc,
                                                      const u16* __restrict__ wq, u16* __restrict__ ows) {
  extern __shared__ __attribute__((aligned(16))) char lds[];
  const int tid = threadIdx.x, wid = __builtin_amdgcn_readfirstlane(tid >> 6), lane = tid & 63;
  const int r = wid & 1, c = wid >> 1;
  const int bid = blockIdx.x;
  const int g = bid & 7, b = bid >> 5, hh = (bid >> 3) & 3, h = g * 4 + hh;
  const int l31 = lane & 31, hf = lane >> 5, q4 = (lane & 15) >> 2, p4 = lane & 3, blk = (lane >> 4) & 1;

  const unsigned ldsb = (unsigned)(uintptr_t)(LAS char*)lds;
  unsigned voffx[4], voffw[4], voffk[4];
#pragma unroll
  for (int i = 0; i < 4; ++i) {
    unsigned row = wid * 16 + (lane >> 4) + 4 * i;
    unsigned ch = (lane & 15) ^ (((lane >> 4) << 2) | i);
    voffx[i] = row * 1024 + ch * 16; voffw[i] = row * 8192 + ch * 16; voffk[i] = row * 256 + ch * 16;
  }
  auto issue = [&](int u) {
    const unsigned dst = ldsb + (u % SLOTS) * UNIT + wid * 4096;
    if (u < 12) {
      int q = ((u / 3) + (b >> 1)) & 3, rr = u % 3;
      if (rr == 0) { const u16* src = x + (size_t)b * 64 * 512 + q * 128;
#pragma unroll
        for (int i = 0; i < 4; ++i) glds16(src, voffx[i], dst + i * 1024); }
      else { int wu = q * 2 + ((rr - 1) ^ (b & 1)); const u16* src = wq + (size_t)wu * 64 * 4096 + h * 128;
#pragma unroll
        for (int i = 0; i < 4; ++i) glds16(src, voffw[i], dst + i * 1024); }
    } else {
      int k = (u - 12 + 2 * hh) & 7; const u16* src = (u < 20 ? kc : vc) + ((size_t)(b * 8 + g) * 512 + k * 64) * 128;
#pragma unroll
      for (int i = 0; i < 4; ++i) glds16(src, voffk[i], dst + i * 1024);
    }
  };
#pragma unroll
  for (int u = 0; u < issued_upto(-1); ++u) issue(u);

  auto rowbase = [&](int R) { return (unsigned)(256 * R + 16 * (hf ^ (((R & 3) << 2) | ((R >> 2) & 3)))); };
  const unsigned xb = rowbase(32 * r + l31);
  const unsigned kb = rowbase(32 * c + l31);
  auto trW = [&](int dt, int t) { return (unsigned)(256 * (8 * hf + 4 * t + q4) + 16 * (((dt ^ q4) << 2) | ((2 * blk + (p4 >> 1)) ^ (2 * hf + t))) + 8 * (p4 & 1)); };
  auto trV = [&](int dt, int t) { return (unsigned)(256 * (32 * c + 8 * t + 4 * hf + q4) + 16 * (((dt ^ q4) << 2) | ((2 * blk + (p4 >> 1)) ^ (2 * t + hf))) + 8 * (p4 & 1)); };

#define STAGE_BEGIN(s)                                              \
  wait_vm<(issued_upto(prev_stage(s)) - (s) - 1) * 4>();           \
  bar();                                                            \
  _Pragma("unroll") for (int u_ = issued_upto(prev_stage(s)); u_ < issued_upto(s); ++u_) issue(u_);

  f32x16 qacc[2] = {};
#define QSTAGE(s) {                                                                                         \
    STAGE_BEGIN(s)                                                                                          \
    const char* xs = lds + (((s) / 3 * 3) % SLOTS) * UNIT;                                                  \
    const char* ws_ = lds + ((s) % SLOTS) * UNIT;                                                           \
    const int khalf = (((s) % 3) - 1) ^ (b & 1);                                                            \
    _Pragma("unroll") for (int ks = 0; ks < 4; ++ks) {                                                      \
      bf16x8 xf = *(const LAS bf16x8*)(LAS char*)(xs + (xb ^ (unsigned)(32 * (khalf * 4 + ks))));                      \
      _Pragma("unroll") for (int i = 0; i < 2; ++i) {                                                       \
        bf16x8 wf = trread2(ws_ + 4096 * ks, trW(2 * c + i, 0), trW(2 * c + i, 1));                          \
        qacc[i] = MFMA32(wf, xf, qacc[i]);                                                                  \
      }                                                                                                     \
    }                                                                                                       \
  }
  QSTAGE(1) QSTAGE(2) QSTAGE(4) QSTAGE(5) QSTAGE(7) QSTAGE(8) QSTAGE(10) QSTAGE(11)
  {
    const int R = 32 * r + l31; const int fR = ((R & 3) << 2) | ((R >> 2) & 3);
#pragma unroll
    for (int i = 0; i < 2; ++i)
#pragma unroll
      for (int gq = 0; gq < 4; ++gq) {
        u32x2 v = {pk2(qacc[i][4 * gq], qacc[i][4 * gq + 1]), pk2(qacc[i][4 * gq + 2], qacc[i][4 * gq + 3])};
        *(LAS u32x2*)(LAS char*)(lds + QIMG + 256 * R + 16 * ((4 * (2 * c + i) + gq) ^ fR) + 8 * hf) = v;
      }
  }
  bf16x8 qf[8];
  f32x16 sacc[8];
#define KSTAGE(s) {                                                                                         \
    STAGE_BEGIN(s)                                                                                          \
    if ((s) == 12) { _Pragma("unroll") for (int ks = 0; ks < 8; ++ks) qf[ks] = *(const LAS bf16x8*)(LAS char*)(lds + QIMG + (xb ^ (unsigned)(32 * ks))); } \
    const char* ks_ = lds + ((s) % SLOTS) * UNIT;                                                           \
    f32x16 a = {};                                                                                          \
    _Pragma("unroll") for (int ks = 0; ks < 8; ++ks) {                                                      \
      bf16x8 kf = *(const LAS bf16x8*)(LAS char*)(ks_ + (kb ^ (unsigned)(32 * ks)));                                   \
      a = MFMA32(kf, qf[ks], a);                                                                            \
    }                                                                                                       \
    sacc[(s) - 12] = a;                                                                                     \
  }
  KSTAGE(12) KSTAGE(13) KSTAGE(14) KSTAGE(15) KSTAGE(16) KSTAGE(17) KSTAGE(18) KSTAGE(19)
  float m = sacc[0][0];
#pragma unroll
  for (int j = 0; j < 8; ++j)
#pragma unroll
    for (int e = 0; e < 16; ++e) m = fmaxf(m, sacc[j][e]);
  { auto rr = __builtin_amdgcn_permlane32_swap(__float_as_uint(m), __float_as_uint(m), false, false);
    m = fmaxf(__uint_as_float(rr[0]), __uint_as_float(rr[1])); }
  float* stat = (float*)(lds + QIMG);
  if (hf == 0) stat[c * 64 + 32 * r + l31] = m;
  constexpr float CS = 0.088388347648318440f * 1.4426950408889634f;
  f32x16 oacc[4] = {};
  float lsum = 0.f, mC = 0.f;
#define VSTAGE(s) {                                                                                         \
    STAGE_BEGIN(s)                                                                                          \
    if ((s) == 20) { m = fmaxf(m, stat[(c ^ 1) * 64 + 32 * r + l31]); mC = -m * CS; }                       \
    const char* vs_ = lds + ((s) % SLOTS) * UNIT;                                                           \
    f32x16 p;                                                                                               \
    _Pragma("unroll") for (int e = 0; e < 16; ++e) { p[e] = __builtin_amdgcn_exp2f(fmaf(sacc[(s) - 20][e], CS, mC)); lsum += p[e]; } \
    _Pragma("unroll") for (int ks = 0; ks < 2; ++ks) {                                                      \
      bf16x8 pf = pack8(p, ks);                                                                             \
      _Pragma("unroll") for (int dt = 0; dt < 4; ++dt) {                                                    \
        bf16x8 vf = trread2(vs_ + 4096 * ks, trV(dt, 0), trV(dt, 1));                                        \
        oacc[dt] = MFMA32(vf, pf, oacc[dt]);                                                                \
      }                                                                                                     \
    }                                                                                                       \
  }
  VSTAGE(20) VSTAGE(21) VSTAGE(22) VSTAGE(23) VSTAGE(24) VSTAGE(25) VSTAGE(26) VSTAGE(27)
  { auto rr = __builtin_amdgcn_permlane32_swap(__float_as_uint(lsum), __float_as_uint(lsum), false, false);
    lsum = __uint_as_float(rr[0]) + __uint_as_float(rr[1]); }
  if (hf == 0) stat[128 + c * 64 + 32 * r + l31] = lsum;
  {
    char* mine = lds + OEX + wid * 8192;
#pragma unroll
    for (int i = 0; i < 2; ++i)
#pragma unroll
      for (int gq = 0; gq < 4; ++gq) {
        const f32x16& t = c ? oacc[i] : oacc[2 + i];
        f32x4 v = {t[4 * gq], t[4 * gq + 1], t[4 * gq + 2], t[4 * gq + 3]};
        *(LAS f32x4*)(LAS char*)(mine + (i * 4 + gq) * 1024 + lane * 16) = v;
      }
  }
  bar();
  {
    const char* other = lds + OEX + (wid ^ 2) * 8192;
    float ltot = lsum + stat[128 + (c ^ 1) * 64 + 32 * r + l31];
    float inv = 1.0f / ltot;
    u16* dst = ows + ((((size_t)(b * 32 + h) * 2 + r) * 4 + 2 * c) * 2) * 512 + lane * 8;
#pragma unroll
    for (int i = 0; i < 2; ++i) {
      f32x16 t = c ? oacc[2 + i] : oacc[i];
#pragma unroll
      for (int gq = 0; gq < 4; ++gq) {
        f32x4 v = *(const LAS f32x4*)(LAS char*)(other + (i * 4 + gq) * 1024 + lane * 16);
#pragma unroll
        for (int e = 0; e < 4; ++e) t[4 * gq + e] = (t[4 * gq + e] + v[e]) * inv;
      }
#pragma unroll
      for (int s = 0; s < 2; ++s) *(bf16x8*)(dst + (i * 2 + s) * 512) = pack8(t, s);
    }
  }
}

constexpr int LDS2 = 8 * 16384;
__global__ __launch_bounds__(512, 1) void oproj_kernel(const u16* __restrict__ ows, const u16* __restrict__ wo, u16* __restrict__ out) {
  extern __shared__ __attribute__((aligned(16))) char lds[];
  const int tid = threadIdx.x, wid = __builtin_amdgcn_readfirstlane(tid >> 6), lane = tid & 63;
  const int bid = blockIdx.x, xc = bid & 7, idx = bid >> 3;
  const int rt = (xc & 3) * 4 + (idx & 3), ct = (xc >> 2) * 8 + (idx >> 2);
  const int b = rt >> 1, r = rt & 1;
  const int l31 = lane & 31, hf = lane >> 5, q4 = (lane & 15) >> 2, p4 = lane & 3, blk = (lane >> 4) & 1;
  char* wl = lds + wid * 16384;
  const unsigned wldst = (unsigned)(uintptr_t)(LAS char*)lds + wid * 16384;
  const unsigned wvoff = (lane >> 2) * 1024 + (lane & 3) * 16;
  auto issue = [&](int half) {
#pragma unroll
    for (int i = 0; i < 16; ++i)
      glds16(wo + ((size_t)wid * 512 + half * 256 + i * 16) * 512 + ct * 32, wvoff, wldst + i * 1024);
  };
  issue(0);
  bf16x8 of[32];
#pragma unroll
  for (int hq = 0; hq < 4; ++hq)
#pragma unroll
    for (int k = 0; k < 8; ++k)
      of[hq * 8 + k] = *(const bf16x8*)(ows + (((size_t)(b * 32 + wid * 4 + hq) * 2 + r) * 8 + k) * 512 + lane * 8);
  const unsigned tb = (unsigned)((4 * hf + q4) * 64 + blk * 32 + p4 * 8);
  f32x16 acc = {};
  wait_vm<0>();
#pragma unroll
  for (int ks = 0; ks < 16; ++ks) {
    bf16x8 wf = trread2(wl + ks * 1024, tb, tb + 512);
    acc = MFMA32(of[ks], wf, acc);
  }
  asm volatile("s_waitcnt lgkmcnt(0)" ::: "memory");
  __builtin_amdgcn_sched_barrier(0);
  issue(1);
  wait_vm<0>();
#pragma unroll
  for (int ks = 0; ks < 16; ++ks) {
    bf16x8 wf = trread2(wl + ks * 1024, tb, tb + 512);
    acc = MFMA32(of[16 + ks], wf, acc);
  }
  asm volatile("s_waitcnt lgkmcnt(0)" ::: "memory");
#pragma unroll
  for (int gq = 0; gq < 4; ++gq) {
    f32x4 v = {acc[4 * gq], acc[4 * gq + 1], acc[4 * gq + 2], acc[4 * gq + 3]};
    *(LAS f32x4*)(LAS char*)(wl + gq * 1024 + lane * 16) = v;
  }
  bar();
  {
    f32x2 s = {0.f, 0.f};
#pragma unroll
    for (int w = 0; w < 8; ++w) { f32x2 v = *(const LAS f32x2*)(LAS char*)(lds + w * 16384 + tid * 8); s += v; }
    const int gq = tid >> 7, ln = (tid >> 1) & 63, e = (tid & 1) * 2;
    const int row = 8 * gq + 4 * (ln >> 5) + e, col = ln & 31;
    u16* o = out + (size_t)(rt * 32 + row) * 512 + ct * 32 + col;
    unsigned pk = pk2(s[0], s[1]);
    o[0] = (u16)(pk & 0xffff);
    o[512] = (u16)(pk >> 16);
  }
}

extern "C" void kernel_launch(void* const* d_in, const int* in_sizes, int n_in, void* d_out, int out_size, void* d_ws, size_t ws_size, hipStream_t stream) {
  (void)hipFuncSetAttribute((const void*)attn_kernel, hipFuncAttributeMaxDynamicSharedMemorySize, LDS1);
  (void)hipFuncSetAttribute((const void*)oproj_kernel, hipFuncAttributeMaxDynamicSharedMemorySize, LDS2);
  attn_kernel<<<256, 256, LDS1, stream>>>((const u16*)d_in[0], (const u16*)d_in[1], (const u16*)d_in[2], (const u16*)d_in[3], (u16*)d_ws);
  oproj_kernel<<<256, 512, LDS2, stream>>>((const u16*)d_ws, (const u16*)d_in[4], (u16*)d_out);
}
```

```cpp
#define MEAS 0
#include <hip/hip_runtime.h>
#include <hip/hip_bf16.h>
#include <utility>

typedef unsigned short u16;
typedef __bf16 bf16x8 __attribute__((ext_vector_type(8)));
typedef __bf16 bf16x2 __attribute__((ext_vector_type(2)));
typedef short s16x4 __attribute__((ext_vector_type(4)));
typedef float f32x16 __attribute__((ext_vector_type(16)));
typedef float f32x4 __attribute__((ext_vector_type(4)));
typedef float f32x2 __attribute__((ext_vector_type(2)));
typedef unsigned u32x4 __attribute__((ext_vector_type(4)));
typedef unsigned u32x2 __attribute__((ext_vector_type(2)));
#define LAS __attribute__((address_space(3)))
#define DI __device__ __forceinline__
#define MFMA32(a, b, c) __builtin_amdgcn_mfma_f32_32x32x16_bf16((a), (b), (c), 0, 0, 0)

DI unsigned pk2(float a, float b) { f32x2 v = {a, b}; bf16x2 r = __builtin_convertvector(v, bf16x2); return __builtin_bit_cast(unsigned, r); }
DI bf16x8 pack8(const f32x16& x, int s) {
  u32x4 p = {pk2(x[8 * s], x[8 * s + 1]), pk2(x[8 * s + 2], x[8 * s + 3]), pk2(x[8 * s + 4], x[8 * s + 5]), pk2(x[8 * s + 6], x[8 * s + 7])};
  return __builtin_bit_cast(bf16x8, p);
}
DI bf16x8 trread2(const char* lds_base, unsigned a0, unsigned a1) {
  s16x4 lo = __builtin_amdgcn_ds_read_tr16_b64_v4i16((LAS s16x4*)(lds_base + a0));
  s16x4 hi = __builtin_amdgcn_ds_read_tr16_b64_v4i16((LAS s16x4*)(lds_base + a1));
  return __builtin_bit_cast(bf16x8, __builtin_shufflevector(lo, hi, 0, 1, 2, 3, 4, 5, 6, 7));
}
DI void glds16(const void* sbase, unsigned voff, unsigned lds_dst) {
  unsigned keep;
  asm volatile("s_mov_b32 %0, m0\n\ts_mov_b32 m0, %3\n\ts_nop 0\n\tglobal_load_lds_dwordx4 %1, %2\n\ts_mov_b32 m0, %0"
               : "=&s"(keep) : "v"(voff), "s"(sbase), "s"(lds_dst) : "memory");
}
template <int N> DI void wait_vm() { asm volatile("s_waitcnt vmcnt(%0)" ::"n"(N) : "memory"); }
DI void bar() { asm volatile("s_waitcnt lgkmcnt(0)" ::: "memory"); __builtin_amdgcn_s_barrier(); asm volatile("" ::: "memory"); }
DI float swapmax(float m) { auto rr = __builtin_amdgcn_permlane32_swap(__float_as_uint(m), __float_as_uint(m), false, false); return fmaxf(__uint_as_float(rr[0]), __uint_as_float(rr[1])); }
DI float swapadd(float m) { auto rr = __builtin_amdgcn_permlane32_swap(__float_as_uint(m), __float_as_uint(m), false, false); return __uint_as_float(rr[0]) + __uint_as_float(rr[1]); }

#define DIST 4
constexpr int UNIT = 16384, SLOTS = 7, NUNITS = 28, NST = 24;
constexpr int QIMG = SLOTS * UNIT;
constexpr int OEX = QIMG + UNIT;
constexpr int LDS1 = OEX + 2 * UNIT;

constexpr int cu(int t) { return t < 8 ? (t / 2) * 3 + 1 + (t & 1) : t + 4; }
constexpr int first_needed(int t) { return t < 8 ? (t / 2) * 3 : t + 4; }
constexpr int issued(int t) {
  int nx = t + 1 < NST ? t + 1 : NST - 1;
  int a = (t < 0 ? 0 : first_needed(nx)) + SLOTS, b2 = cu(nx) + 1 + DIST;
  int lim = a < b2 ? a : b2; return lim < NUNITS ? lim : NUNITS;
}

#define STAMP(i)
#define MSTAMP(k, i) do { unsigned long long t_ = __builtin_amdgcn_s_memrealtime(); if (tid == 0) ((unsigned long long*)((char*)ows + (16u << 20)))[((k) * 256 + bid) * 4 + (i)] = t_; } while (0)

template <typename F, int... I> DI void static_for_impl(F&& f, std::integer_sequence<int, I...>) { (f(std::integral_constant<int, I>{}), ...); }
template <int N, typename F> DI void static_for(F&& f) { static_for_impl(f, std::make_integer_sequence<int, N>{}); }

__global__ __launch_bounds__(256, 1) void attn_kernel(const u16* __restrict__ x, const u16* __restrict__ kc, const u16* __restrict__ vc,
                                                      const u16* __restrict__ wq, u16* __restrict__ ows) {
  extern __shared__ __attribute__((aligned(16))) char lds[];
  const int tid = threadIdx.x, wid = __builtin_amdgcn_readfirstlane(tid >> 6), lane = tid & 63;
  const int r = wid & 1, c = wid >> 1;
  const int bid = blockIdx.x;
  const int g = bid & 7, b = bid >> 5, hh = (bid >> 3) & 3, h = g * 4 + hh;
  const int l31 = lane & 31, hf = lane >> 5, q4 = (lane & 15) >> 2, p4 = lane & 3, blk = (lane >> 4) & 1;
  MSTAMP(0, 0);
  const unsigned ldsb = (unsigned)(uintptr_t)(LAS char*)lds;
  unsigned voffx[4], voffw[4], voffk[4];
#pragma unroll
  for (int i = 0; i < 4; ++i) {
    unsigned row = wid * 16 + (lane >> 4) + 4 * i;
    unsigned ch = (lane & 15) ^ (((lane >> 4) << 2) | i);
    voffx[i] = row * 1024 + ch * 16; voffw[i] = row * 8192 + ch * 16; voffk[i] = row * 256 + ch * 16;
  }
  auto issue = [&](int u) {
    const unsigned dst = ldsb + (u % SLOTS) * UNIT + wid * 4096;
    if (u < 12) {
      int q = ((u / 3) + (b >> 1)) & 3, rr = u % 3;
      if (rr == 0) { const u16* src = x + (size_t)b * 64 * 512 + q * 128;
#pragma unroll
        for (int i = 0; i < 4; ++i) glds16(src, voffx[i], dst + i * 1024); }
      else { int wu = q * 2 + ((rr - 1) ^ (b & 1)); const u16* src = wq + (size_t)wu * 64 * 4096 + h * 128;
#pragma unroll
        for (int i = 0; i < 4; ++i) glds16(src, voffw[i], dst + i * 1024); }
    } else {
      int k = (u - 12 + 2 * hh) & 7; const u16* src = (u < 20 ? kc : vc) + ((size_t)(b * 8 + g) * 512 + k * 64) * 128;
#pragma unroll
      for (int i = 0; i < 4; ++i) glds16(src, voffk[i], dst + i * 1024);
    }
  };
  constexpr int ISS0 = cu(0) + 1 + DIST < SLOTS ? cu(0) + 1 + DIST : SLOTS;
#pragma unroll
  for (int u = 0; u < ISS0; ++u) issue(u);

  auto rowbase = [&](int R) { return (unsigned)(256 * R + 16 * (hf ^ (((R & 3) << 2) | ((R >> 2) & 3)))); };
  const unsigned xb = rowbase(32 * r + l31);
  const unsigned kb = rowbase(32 * c + l31);
  auto trW = [&](int dt, int t) { return (unsigned)(256 * (8 * hf + 4 * t + q4) + 16 * (((dt ^ q4) << 2) | ((2 * blk + (p4 >> 1)) ^ (2 * hf + t))) + 8 * (p4 & 1)); };
  auto trV = [&](int dt, int t) { return (unsigned)(256 * (32 * c + 8 * t + 4 * hf + q4) + 16 * (((dt ^ q4) << 2) | ((2 * blk + (p4 >> 1)) ^ (2 * t + hf))) + 8 * (p4 & 1)); };
  const unsigned xk = (unsigned)((b & 1) * 128);

  bf16x8 fa[2][8], fx[2][4], qf[8], pf[8][2];
  f32x16 qacc[2] = {}, sacc[8], oA[4] = {}, oB[4] = {};
  float m1 = -3.0e38f, m2 = -3.0e38f, lA = 0.f, lB = 0.f, alpha = 0.f, mCA = 0.f, mCB = 0.f;
  constexpr float CS = 0.088388347648318440f * 1.4426950408889634f;

  auto read_frags = [&](auto T) {
    constexpr int t = T;
    const char* ub = lds + (cu(t) % SLOTS) * UNIT;
    if constexpr (t < 8) {
      const char* xs = lds + (first_needed(t) % SLOTS) * UNIT;
#pragma unroll
      for (int ks = 0; ks < 4; ++ks) {
        fx[t & 1][ks] = *(const LAS bf16x8*)(LAS char*)(xs + (xb ^ xk ^ (unsigned)(32 * ((t & 1) * 4 + ks))));
#pragma unroll
        for (int i = 0; i < 2; ++i) fa[t & 1][ks * 2 + i] = trread2(ub + 4096 * ks, trW(2 * c + i, 0), trW(2 * c + i, 1));
      }
    } else if constexpr (t < 16) {
#pragma unroll
      for (int ks = 0; ks < 8; ++ks) fa[t & 1][ks] = *(const LAS bf16x8*)(LAS char*)(ub + (kb ^ (unsigned)(32 * ks)));
    } else {
#pragma unroll
      for (int ks = 0; ks < 2; ++ks)
#pragma unroll
        for (int dt = 0; dt < 4; ++dt) fa[t & 1][ks * 4 + dt] = trread2(ub + 4096 * ks, trV(dt, 0), trV(dt, 1));
    }
  };
  auto exps = [&](auto J) {
    constexpr int j = J;
    f32x16 p;
    const float mc = j < 4 ? mCA : mCB;
    float ls = 0.f;
#pragma unroll
    for (int e = 0; e < 16; ++e) { p[e] = __builtin_amdgcn_exp2f(fmaf(sacc[j][e], CS, mc)); ls += p[e]; }
    if (j < 4) lA += ls; else lB += ls;
    pf[j][0] = pack8(p, 0); pf[j][1] = pack8(p, 1);
  };
  auto tilemax = [&](auto J) {
    constexpr int j = J;
    float mm = sacc[j][0];
#pragma unroll
    for (int e = 1; e < 16; ++e) mm = fmaxf(mm, sacc[j][e]);
    if (j < 4) m1 = fmaxf(m1, mm); else m2 = fmaxf(m2, mm);
  };

  wait_vm<(ISS0 - cu(0) - 1) * 4>();
  bar();
  MSTAMP(0, 2);
  read_frags(std::integral_constant<int, 0>{});

  static_for<NST>([&](auto T) {
    constexpr int t = T;
    constexpr int prev = t == 0 ? ISS0 : issued(t - 1);
    STAMP(2 + 2 * t);
    if constexpr (t + 1 < NST) wait_vm<(prev - cu(t + 1) - 1) * 4>();
    bar();
    STAMP(3 + 2 * t);
#pragma unroll
    for (int u = prev; u < issued(t); ++u) issue(u);
    if constexpr (t == 8) {
#pragma unroll
      for (int ks = 0; ks < 8; ++ks) qf[ks] = *(const LAS bf16x8*)(LAS char*)(lds + QIMG + (xb ^ (unsigned)(32 * ks)));
    }
    if constexpr (t + 1 < NST) read_frags(std::integral_constant<int, t + 1>{});
    if constexpr (t >= 9 && t <= 16) tilemax(std::integral_constant<int, t - 9>{});
    if constexpr (t == 12) { m1 = swapmax(m1); mCA = -m1 * CS; }
    if constexpr (t == 16) { m2 = fmaxf(swapmax(m2), m1); mCB = -m2 * CS; alpha = __builtin_amdgcn_exp2f((m1 - m2) * CS); }
    if constexpr (t >= 12 && t <= 19) exps(std::integral_constant<int, t - 12>{});
    if constexpr (t < 8) {
#pragma unroll
      for (int ks = 0; ks < 4; ++ks)
#pragma unroll
        for (int i = 0; i < 2; ++i) qacc[i] = MFMA32(fa[t & 1][ks * 2 + i], fx[t & 1][ks], qacc[i]);
      if constexpr (t == 7) {
        const int R = 32 * r + l31; const int fR = ((R & 3) << 2) | ((R >> 2) & 3);
#pragma unroll
        for (int i = 0; i < 2; ++i)
#pragma unroll
          for (int gq = 0; gq < 4; ++gq) {
            u32x2 v = {pk2(qacc[i][4 * gq], qacc[i][4 * gq + 1]), pk2(qacc[i][4 * gq + 2], qacc[i][4 * gq + 3])};
            *(LAS u32x2*)(LAS char*)(lds + QIMG + 256 * R + 16 * ((4 * (2 * c + i) + gq) ^ fR) + 8 * hf) = v;
          }
      }
    } else if constexpr (t < 16) {
      f32x16 a = {};
#pragma unroll
      for (int ks = 0; ks < 8; ++ks) a = MFMA32(fa[t & 1][ks], qf[ks], a);
      sacc[t - 8] = a;
    } else {
      constexpr int j = t - 16;
#pragma unroll
      for (int ks = 0; ks < 2; ++ks)
#pragma unroll
        for (int dt = 0; dt < 4; ++dt) {
          if constexpr (j < 4) oA[dt] = MFMA32(fa[t & 1][ks * 4 + dt], pf[j][ks], oA[dt]);
          else oB[dt] = MFMA32(fa[t & 1][ks * 4 + dt], pf[j][ks], oB[dt]);
        }
    }
  });
  MSTAMP(0, 3);
  float lw = swapadd(fmaf(lA, alpha, lB));
  float* stat = (float*)(lds + QIMG);
  if (hf == 0) { stat[c * 64 + 32 * r + l31] = m2; stat[128 + c * 64 + 32 * r + l31] = lw; }
  {
    char* mine = lds + OEX + wid * 8192;
#pragma unroll
    for (int i = 0; i < 2; ++i)
#pragma unroll
      for (int gq = 0; gq < 4; ++gq) {
        const f32x16& ta = c ? oA[i] : oA[2 + i];
        const f32x16& tb = c ? oB[i] : oB[2 + i];
        f32x4 v;
#pragma unroll
        for (int e = 0; e < 4; ++e) v[e] = fmaf(ta[4 * gq + e], alpha, tb[4 * gq + e]);
        *(LAS f32x4*)(LAS char*)(mine + (i * 4 + gq) * 1024 + lane * 16) = v;
      }
  }
  STAMP(58);
  bar();
  STAMP(59);
  {
    const char* other = lds + OEX + (wid ^ 2) * 8192;
    const float mo = stat[(c ^ 1) * 64 + 32 * r + l31], lo = stat[128 + (c ^ 1) * 64 + 32 * r + l31];
    const float M = fmaxf(m2, mo);
    const float bs = __builtin_amdgcn_exp2f((m2 - M) * CS), bo = __builtin_amdgcn_exp2f((mo - M) * CS);
    const float inv = 1.0f / (lw * bs + lo * bo);
    const float fsA = alpha * bs * inv, fsB = bs * inv, fo = bo * inv;
    u16* dst = ows + ((((size_t)(b * 32 + h) * 2 + r) * 4 + 2 * c) * 2) * 512 + lane * 8;
#pragma unroll
    for (int i = 0; i < 2; ++i) {
      const f32x16& ta = c ? oA[2 + i] : oA[i];
      const f32x16& tb = c ? oB[2 + i] : oB[i];
      f32x16 t;
#pragma unroll
      for (int gq = 0; gq < 4; ++gq) {
        f32x4 v = *(const LAS f32x4*)(LAS char*)(other + (i * 4 + gq) * 1024 + lane * 16);
#pragma unroll
        for (int e = 0; e < 4; ++e) t[4 * gq + e] = fmaf(ta[4 * gq + e], fsA, fmaf(tb[4 * gq + e], fsB, v[e] * fo));
      }
#pragma unroll
      for (int s = 0; s < 2; ++s) *(bf16x8*)(dst + (i * 2 + s) * 512) = pack8(t, s);
    }
  }
  MSTAMP(0, 1);
}

constexpr int LDS2 = 8 * 16384;
__global__ __launch_bounds__(512, 1) void oproj_kernel(u16* __restrict__ ows, const u16* __restrict__ wo, u16* __restrict__ out) {
  extern __shared__ __attribute__((aligned(16))) char lds[];
  const int tid = threadIdx.x, wid = __builtin_amdgcn_readfirstlane(tid >> 6), lane = tid & 63;
  const int bid = blockIdx.x, xc = bid & 7, idx = bid >> 3;
  const int rt = (xc & 3) * 4 + (idx & 3), ct = (xc >> 2) * 8 + (idx >> 2);
  const int b = rt >> 1, r = rt & 1;
  const int hf = lane >> 5, q4 = (lane & 15) >> 2, p4 = lane & 3, blk = (lane >> 4) & 1;
  MSTAMP(1, 0);
  char* wl = lds + wid * 16384;
  const unsigned wldst = (unsigned)(uintptr_t)(LAS char*)lds + wid * 16384;
  const unsigned wvoff = (lane >> 2) * 1024 + (lane & 3) * 16;
  auto issue = [&](int half) {
#pragma unroll
    for (int i = 0; i < 16; ++i)
      glds16(wo + ((size_t)wid * 512 + half * 256 + i * 16) * 512 + ct * 32, wvoff, wldst + i * 1024);
  };
  issue(0);
  bf16x8 of[32];
#pragma unroll
  for (int hq = 0; hq < 4; ++hq)
#pragma unroll
    for (int k = 0; k < 8; ++k)
      of[hq * 8 + k] = *(const bf16x8*)(ows + (((size_t)(b * 32 + wid * 4 + hq) * 2 + r) * 8 + k) * 512 + lane * 8);
  const unsigned tb = (unsigned)((4 * hf + q4) * 64 + blk * 32 + p4 * 8);
  f32x16 acc = {};
  wait_vm<0>();
#pragma unroll
  for (int ks = 0; ks < 16; ++ks) {
    bf16x8 wf = trread2(wl + ks * 1024, tb, tb + 512);
    acc = MFMA32(of[ks], wf, acc);
  }
  asm volatile("s_waitcnt lgkmcnt(0)" ::: "memory");
  __builtin_amdgcn_sched_barrier(0);
  issue(1);
  wait_vm<0>();
#pragma unroll
  for (int ks = 0; ks < 16; ++ks) {
    bf16x8 wf = trread2(wl + ks * 1024, tb, tb + 512);
    acc = MFMA32(of[16 + ks], wf, acc);
  }
  asm volatile("s_waitcnt lgkmcnt(0)" ::: "memory");
#pragma unroll
  for (int gq = 0; gq < 4; ++gq) {
    f32x4 v = {acc[4 * gq], acc[4 * gq + 1], acc[4 * gq + 2], acc[4 * gq + 3]};
    *(LAS f32x4*)(LAS char*)(wl + gq * 1024 + lane * 16) = v;
  }
  bar();
  {
    f32x2 s = {0.f, 0.f};
#pragma unroll
    for (int w = 0; w < 8; ++w) { f32x2 v = *(const LAS f32x2*)(LAS char*)(lds + w * 16384 + tid * 8); s += v; }
    const int gq = tid >> 7, ln = (tid >> 1) & 63, e = (tid & 1) * 2;
    const int row = 8 * gq + 4 * (ln >> 5) + e, col = ln & 31;
    u16* o = out + (size_t)(rt * 32 + row) * 512 + ct * 32 + col;
    unsigned pk = pk2(s[0], s[1]);
    o[0] = (u16)(pk & 0xffff);
    o[512] = (u16)(pk >> 16);
  }
  MSTAMP(1, 1);
}
__global__ void spin_kernel(const unsigned long long* st) {
  __shared__ unsigned long long sh[8][256];
  const int t = threadIdx.x;
  unsigned long long t0 = __builtin_amdgcn_s_memrealtime();
  for (int k = 0; k < 2; ++k) for (int i = 0; i < 4; ++i) sh[k * 4 + i][t] = st[(k * 256 + t) * 4 + i];
  __syncthreads();
  if (t == 0) {
    unsigned long long mn[8], mx[8]; unsigned long long sum1 = 0, sum2 = 0, sum3 = 0;
    for (int q = 0; q < 8; ++q) { mn[q] = ~0ull; mx[q] = 0; for (int j = 0; j < 256; ++j) { mn[q] = sh[q][j] < mn[q] ? sh[q][j] : mn[q]; mx[q] = sh[q][j] > mx[q] ? sh[q][j] : mx[q]; } }
    for (int j = 0; j < 256; ++j) { sum1 += sh[2][j] - sh[0][j]; sum2 += sh[3][j] - sh[2][j]; sum3 += sh[5][j] - sh[4][j]; }
    unsigned long long X = 0;
    if (MEAS == 1) X = mx[1] - mn[0];
    if (MEAS == 2) X = mx[5] - mn[4];
    if (MEAS == 3) X = mn[4] - mx[1];
    if (MEAS == 4) X = sum1 / 256;
    if (MEAS == 5) X = sum2 / 256;
    if (MEAS == 6) X = mx[0] - mn[0];
    if (MEAS == 7) X = sum3 / 256;
    if (MEAS == 8) X = t0 - mx[5];
    if (X > 20000) X = 20000;
    unsigned long long tb = __builtin_amdgcn_s_memrealtime();
    while (__builtin_amdgcn_s_memrealtime() - tb < 4 * X) __builtin_amdgcn_s_sleep(1);
  }
}

extern "C" void kernel_launch(void* const* d_in, const int* in_sizes, int n_in, void* d_out, int out_size, void* d_ws, size_t ws_size, hipStream_t stream) {
  (void)hipFuncSetAttribute((const void*)attn_kernel, hipFuncAttributeMaxDynamicSharedMemorySize, LDS1);
  (void)hipFuncSetAttribute((const void*)oproj_kernel, hipFuncAttributeMaxDynamicSharedMemorySize, LDS2);
  attn_kernel<<<256, 256, LDS1, stream>>>((const u16*)d_in[0], (const u16*)d_in[1], (const u16*)d_in[2], (const u16*)d_in[3], (u16*)d_ws);
  oproj_kernel<<<256, 512, LDS2, stream>>>((u16*)d_ws, (const u16*)d_in[4], (u16*)d_out);
  spin_kernel<<<1, 256, 0, stream>>>((const unsigned long long*)((char*)d_ws + (16u << 20)));
}
```

```cpp
#include <hip/hip_runtime.h>
#include <hip/hip_bf16.h>
#include <utility>

typedef unsigned short u16;
typedef __bf16 bf16x8 __attribute__((ext_vector_type(8)));
typedef __bf16 bf16x2 __attribute__((ext_vector_type(2)));
typedef short s16x4 __attribute__((ext_vector_type(4)));
typedef float f32x16 __attribute__((ext_vector_type(16)));
typedef float f32x4 __attribute__((ext_vector_type(4)));
typedef float f32x2 __attribute__((ext_vector_type(2)));
typedef unsigned u32x4 __attribute__((ext_vector_type(4)));
typedef unsigned u32x2 __attribute__((ext_vector_type(2)));
#define LAS __attribute__((address_space(3)))
#define GAS __attribute__((address_space(1)))
typedef GAS unsigned gu32;
#define DI __device__ __forceinline__
#define MFMA32(a, b, c) __builtin_amdgcn_mfma_f32_32x32x16_bf16((a), (b), (c), 0, 0, 0)
#define RLX_AGENT __ATOMIC_RELAXED, __HIP_MEMORY_SCOPE_AGENT
#define SB() __builtin_amdgcn_sched_barrier(0)

DI unsigned pk2(float a, float b) { f32x2 v = {a, b}; bf16x2 r = __builtin_convertvector(v, bf16x2); return __builtin_bit_cast(unsigned, r); }
DI bf16x8 pack8(const f32x16& x, int s) {
  u32x4 p = {pk2(x[8 * s], x[8 * s + 1]), pk2(x[8 * s + 2], x[8 * s + 3]), pk2(x[8 * s + 4], x[8 * s + 5]), pk2(x[8 * s + 6], x[8 * s + 7])};
  return __builtin_bit_cast(bf16x8, p);
}
DI bf16x8 trread2(unsigned a0, unsigned a1) {
  s16x4 lo = __builtin_amdgcn_ds_read_tr16_b64_v4i16((LAS s16x4*)a0);
  s16x4 hi = __builtin_amdgcn_ds_read_tr16_b64_v4i16((LAS s16x4*)a1);
  return __builtin_bit_cast(bf16x8, __builtin_shufflevector(lo, hi, 0, 1, 2, 3, 4, 5, 6, 7));
}
DI bf16x8 ldsr16(unsigned a) { return *(const LAS bf16x8*)a; }
DI void glds16(const void* sbase, unsigned voff, unsigned lds_dst) {
  unsigned keep;
  asm volatile("s_mov_b32 %0, m0\n\ts_mov_b32 m0, %3\n\ts_nop 0\n\tglobal_load_lds_dwordx4 %1, %2\n\ts_mov_b32 m0, %0"
               : "=&s"(keep) : "v"(voff), "s"(sbase), "s"(lds_dst) : "memory");
}
template <int N> DI void wait_vm() { asm volatile("s_waitcnt vmcnt(%0)" ::"n"(N) : "memory"); }
DI void rawbar() { asm volatile("" ::: "memory"); __builtin_amdgcn_s_barrier(); asm volatile("" ::: "memory"); }
#define STBAR() rawbar()
DI void lgkmbar() { asm volatile("s_waitcnt lgkmcnt(0)" ::: "memory"); __builtin_amdgcn_s_barrier(); asm volatile("" ::: "memory"); }
DI float swapmax(float m) { auto rr = __builtin_amdgcn_permlane32_swap(__float_as_uint(m), __float_as_uint(m), false, false); return fmaxf(__uint_as_float(rr[0]), __uint_as_float(rr[1])); }
DI float swapadd(float m) { auto rr = __builtin_amdgcn_permlane32_swap(__float_as_uint(m), __float_as_uint(m), false, false); return __uint_as_float(rr[0]) + __uint_as_float(rr[1]); }
DI unsigned opaque(unsigned v) { asm volatile("" : "+v"(v)); return v; }
DI float pinf(float v) { asm volatile("" : "+v"(v)); return v; }
DI int fresh_lane() { int l; asm volatile("v_mbcnt_lo_u32_b32 %0, -1, 0\n\tv_mbcnt_hi_u32_b32 %0, -1, %0" : "=v"(l)); return l; }
DI int swz(int R) { return ((R & 3) << 2) | ((R >> 2) & 3); }
DI unsigned rowbase(int R, int hf) { return (unsigned)((R >> 2) * 8192 + (R & 3) * 256 + 16 * (hf ^ swz(R))); }
DI unsigned trW(int lane, int dt, int t) {
  const int hf = lane >> 5, q4 = (lane & 15) >> 2, p4 = lane & 3, blk = (lane >> 4) & 1;
  return (unsigned)(2 * hf * 8192 + q4 * 256 + 16 * (((dt ^ q4) << 2) | ((2 * blk + (p4 >> 1)) ^ (2 * hf + t))) + 8 * (p4 & 1));
}
DI unsigned trV(int lane, int dt, int t) {
  const int hf = lane >> 5, q4 = (lane & 15) >> 2, p4 = lane & 3, blk = (lane >> 4) & 1;
  return (unsigned)(hf * 8192 + q4 * 256 + 16 * (((dt ^ q4) << 2) | ((2 * blk + (p4 >> 1)) ^ (2 * t + hf))) + 8 * (p4 & 1));
}

template <typename F, int... I> DI void static_for_impl(F&& f, std::integer_sequence<int, I...>) { (f(std::integral_constant<int, I>{}), ...); }
template <int N, typename F> DI void static_for(F&& f) { static_for_impl(f, std::make_integer_sequence<int, N>{}); }

constexpr int SLOTS = 8, NUNITS = 36, NST = 24;
constexpr int AUX = 131072;
constexpr int LDSB = AUX + 32768;
#define WLA 4
constexpr int cu(int t) { return t < 8 ? (t / 2) * 3 + 1 + (t & 1) : t + 4; }
constexpr int xu(int t) { return (t / 2) * 3; }
constexpr int lastuse(int u) { return u < 12 ? (u % 3 == 0 ? (u / 3) * 2 + 1 : (u / 3) * 2 + (u % 3) - 1) : u - 4; }
constexpr int issued(int t) {
  int v = 0;
  while (v < NUNITS && (v < SLOTS || lastuse(v - SLOTS) <= t - 2)) ++v;
  return v;
}
constexpr unsigned WS_O = 0, WS_P = 4u << 20, WS_F1 = 12u << 20, WS_F2 = WS_F1 + 65536, WS_EP = WS_F2 + 65536;

enum { OP_MAX, OP_CHK, OP_LS, OP_FX, OP_ADD, OP_CVT };
struct VOp { int type, tile, idx; };
struct VSched { VOp op[512]; int start[194]; };
#define BQK 26
#define BPV 23
constexpr VSched make_sched() {
  VSched s{};
  VOp ex[8][48] = {}; int nex = 0;
  for (int j = 0; j < 8; ++j) {
    int k = 0;
    if (j > 0) ex[j][k++] = VOp{OP_LS, j, 0};
    for (int e = 0; e < 16; ++e) {
      ex[j][k++] = VOp{OP_FX, j, e};
      if (e >= 1) ex[j][k++] = VOp{OP_ADD, j, e - 1};
      if (e >= 2 && (e & 1) == 0) ex[j][k++] = VOp{OP_CVT, j, (e - 2) / 2};
    }
    ex[j][k++] = VOp{OP_ADD, j, 15};
    ex[j][k++] = VOp{OP_CVT, j, 7};
    ex[j][k] = VOp{-1, 0, 0};
    nex = k;
  }
  (void)nex;
  int jm = 0, km = 0, je = 0, ke = 0, no = 0;
  for (int n = 0; n < 192; ++n) {
    s.start[n] = no;
    if (n < 64) continue;
    int bud = n < 128 ? BQK : BPV;
    while (jm < 8 && n >= 64 + 8 * jm + 9 && bud > 0) {
      if (km < 8) { s.op[no++] = VOp{OP_MAX, jm, km}; ++km; bud -= 4; }
      else { s.op[no++] = VOp{OP_CHK, jm, 0}; ++jm; km = 0; bud -= (jm == 1 ? 20 : 8); bud = 0; }
    }
    while (je < jm && je < 8) {
      VOp o = ex[je][ke];
      int cost = o.type == OP_FX ? 12 : 4;
      bool first_half = (o.type == OP_CVT) ? o.idx < 4 : (o.type == OP_FX ? o.idx <= 8 : (o.type == OP_ADD ? o.idx < 8 : true));
      int dl = 128 + 8 * je + (first_half ? 0 : 4) - 2;
      if (bud < cost - 2 && n < dl) break;
      s.op[no++] = o; bud -= cost; ++ke;
      if (ex[je][ke].type < 0) { ++je; ke = 0; }
    }
  }
  s.start[192] = no; s.start[193] = (je == 8 && jm == 8) ? 1 : 0;
  return s;
}
constexpr VSched VS = make_sched();
static_assert(VS.start[193] == 1, "schedule incomplete");
constexpr int last_vslot() { int l = 0; for (int n = 0; n < 192; ++n) if (VS.start[n + 1] > VS.start[n]) l = n; return l; }
constexpr int SSLOT = last_vslot() + 1;
static_assert(SSLOT <= 190 && SSLOT >= 184 - WLA, "stats slot out of range");

#define STAMP(i)
#define CSTAMP(i)

__global__ __launch_bounds__(512, 2) void gqa_kernel(const u16* __restrict__ x, const u16* __restrict__ kc, const u16* __restrict__ vc,
                                                     const u16* __restrict__ wq, const u16* __restrict__ wo, char* __restrict__ ws, u16* __restrict__ out) {
  extern __shared__ __attribute__((aligned(16))) char lds[];
  const int wid8 = __builtin_amdgcn_readfirstlane(threadIdx.x >> 6);
  const int wid = wid8 & 3;
  const int bid = blockIdx.x;
  const int g = bid & 7, b = bid >> 5, hh = (bid >> 3) & 3, h = g * 4 + hh;
  const unsigned ldsb = (unsigned)(uintptr_t)(LAS char*)lds;
  gu32* const epw = (gu32*)(ws + WS_EP);

  if (wid8 >= 4) {
    const int lane = fresh_lane();
    unsigned voffx[4], voffw[4], voffk[4];
#pragma unroll
    for (int i = 0; i < 4; ++i) {
      unsigned row = wid * 16 + (lane >> 4) + 4 * i;
      unsigned ch = (lane & 15) ^ (((lane >> 4) << 2) | i);
      voffx[i] = row * 1024 + ch * 16; voffw[i] = row * 8192 + ch * 16; voffk[i] = row * 256 + ch * 16;
    }
    auto issue = [&](int u) {
      const unsigned dst = ldsb + (u % SLOTS) * 1024 + wid * 32768;
      if (u < 12) {
        int q = ((u / 3) + (b >> 1)) & 3, rr = u % 3;
        if (rr == 0) { const u16* src = x + (size_t)b * 64 * 512 + q * 128;
#pragma unroll
          for (int i = 0; i < 4; ++i) glds16(src, voffx[i], dst + i * 8192); }
        else { int wu = q * 2 + ((rr - 1) ^ (b & 1)); const u16* src = wq + (size_t)wu * 64 * 4096 + h * 128;
#pragma unroll
          for (int i = 0; i < 4; ++i) glds16(src, voffw[i], dst + i * 8192); }
      } else if (u < 28) {
        int k = (u - 12 + 2 * hh) & 7; const u16* src = (u < 20 ? kc : vc) + ((size_t)(b * 8 + g) * 512 + k * 64) * 128;
#pragma unroll
        for (int i = 0; i < 4; ++i) glds16(src, voffk[i], dst + i * 8192);
      } else {
        int k = (u - 28 + 2 * hh) & 7; const u16* src = wo + ((size_t)g * 512 + k * 64) * 512 + hh * 128;
#pragma unroll
        for (int i = 0; i < 4; ++i) glds16(src, voffx[i], dst + i * 8192);
      }
    };
#define LST(i)
    LST(31);
#pragma unroll
    for (int u = 0; u < SLOTS; ++u) issue(u);
    static_for<NST>([&](auto T) {
      constexpr int t = T;
      constexpr int prev = t == 0 ? SLOTS : issued(t - 1), cur = issued(t);
      wait_vm<(prev - cu(t) - 1) * 4>();
      if constexpr (t == 0) rawbar(); else STBAR();
      LST(t);
#pragma unroll
      for (int u = prev; u < cur; ++u) issue(u);
      if constexpr (t == 8) { rawbar(); LST(24); }
    });
    rawbar();
    LST(25);
    rawbar();
    LST(26);
    constexpr int ILAST = issued(NST - 1);
#pragma unroll
    for (int u = ILAST; u < NUNITS; ++u) issue(u);
    wait_vm<(NUNITS - 30) * 4>();
    rawbar();
    LST(27);
    wait_vm<0>();
    rawbar();
    LST(28);
    if (bid == 0 && wid == 0) {
      const unsigned tag = (unsigned)__builtin_amdgcn_readfirstlane(__hip_atomic_load(epw, RLX_AGENT)) + 1u;
      gu32* f1 = (gu32*)(ws + WS_F1) + lane * 32;
      for (unsigned spins = 0; spins < (1u << 17); ++spins) {
        bool ok = true;
#pragma unroll
        for (int i = 0; i < 4; ++i) ok &= __hip_atomic_load(f1 + i, RLX_AGENT) == tag;
        if (__all(ok)) break;
        __builtin_amdgcn_s_sleep(2);
      }
      if (lane == 0) __hip_atomic_store(epw, tag, RLX_AGENT);
    }
    return;
  }

  __builtin_amdgcn_s_setprio(2);
  const int r = wid & 1, c = wid >> 1;
  STAMP(0);
  const unsigned epv = __hip_atomic_load(epw, RLX_AGENT);
  unsigned tag = 0;
  unsigned xa[8], wa[2][2][2], ka[8], va[4][2];
  {
    const int lane = fresh_lane();
    const unsigned xb = (ldsb + rowbase(32 * r + (lane & 31), lane >> 5)) ^ (unsigned)((b & 1) * 128);
#pragma unroll
    for (int j = 0; j < 8; ++j) xa[j] = opaque(xb ^ (unsigned)(32 * j));
#pragma unroll
    for (int i = 0; i < 2; ++i)
#pragma unroll
      for (int t = 0; t < 2; ++t) { wa[i][t][0] = opaque(ldsb + trW(lane, 2 * c + i, t)); wa[i][t][1] = opaque(ldsb + trW(lane, 2 * c + i, t) + 65536u); }
  }

  bf16x8 fr[192], fx[32], qf[8], pf[8][2];
  f32x16 qacc[2] = {}, sacc[8], oacc[4] = {};
  float pv[8][16];
  unsigned pw[8][8];
  float mt[8], mref = 0.f, mC[8], alph[8], lsum = 0.f;
  bool resc[8];
  constexpr float CS = 0.088388347648318440f * 1.4426950408889634f;
  constexpr float TLAZY = 40.0f / CS;

  auto qs_ks = [](int n) { return n < 56 ? (n % 8) / 2 : (n % 4); };
  auto qs_i = [](int n) { return n < 56 ? n % 2 : (n % 8) / 4; };
  auto issue_reads = [&](auto N) {
    constexpr int n = N, t = n / 8;
    constexpr unsigned so = (cu(t) % SLOTS) * 1024;
    if constexpr (n < 64) {
      constexpr int ks = qs_ks(n), i = qs_i(n);
      if constexpr (i == 0) fx[t * 4 + ks] = ldsr16(xa[(t & 1) * 4 + ks] + (xu(t) % SLOTS) * 1024);
      fr[n] = trread2(wa[i][0][ks >> 1] + so + (4 * (ks & 1)) * 8192, wa[i][1][ks >> 1] + so + (4 * (ks & 1) + 1) * 8192);
    } else if constexpr (n < 128) {
      fr[n] = ldsr16(ka[n % 8] + so);
    } else {
      constexpr int ks = (n % 8) / 4, dt = n % 4;
      fr[n] = trread2(va[dt][0] + so + (4 * ks) * 8192, va[dt][1] + so + (4 * ks + 2) * 8192);
    }
  };
  auto qwrite = [&](int i) {
    const int lane = fresh_lane(), hf = lane >> 5;
    const int R = 32 * r + (lane & 31);
#pragma unroll
    for (int gq = 0; gq < 4; ++gq) {
      u32x2 v = {pk2(qacc[i][4 * gq], qacc[i][4 * gq + 1]), pk2(qacc[i][4 * gq + 2], qacc[i][4 * gq + 3])};
      *(LAS u32x2*)(ldsb + AUX + 256 * R + 16 * ((4 * (2 * c + i) + gq) ^ swz(R)) + 8 * hf) = v;
    }
  };
  auto vop = [&](auto K) {
    constexpr VOp o = VS.op[(int)K];
    constexpr int j = o.tile, e = o.idx;
    if constexpr (o.type == OP_MAX) {
      if constexpr (e == 0) mt[j] = fmaxf(sacc[j][0], sacc[j][1]);
      else mt[j] = fmaxf(fmaxf(mt[j], sacc[j][2 * e]), sacc[j][2 * e + 1]);
    } else if constexpr (o.type == OP_CHK) {
      if constexpr (j == 0) { mref = swapmax(mt[0]); mC[0] = -mref * CS; }
      else {
        mC[j] = mC[j - 1]; alph[j] = 1.0f; resc[j] = false;
        if (__builtin_expect(__builtin_amdgcn_ballot_w64(mt[j] > mref + TLAZY) != 0, 0)) {
          const float mn = fmaxf(mref, swapmax(mt[j]));
          alph[j] = __builtin_amdgcn_exp2f((mref - mn) * CS);
          mref = mn; mC[j] = -mn * CS; resc[j] = true;
        }
      }
    } else if constexpr (o.type == OP_LS) {
      lsum = pinf(lsum * alph[j]);
    } else if constexpr (o.type == OP_FX) {
      pv[j][e] = pinf(__builtin_amdgcn_exp2f(fmaf(sacc[j][e], CS, mC[j])));
    } else if constexpr (o.type == OP_ADD) {
      lsum = pinf(lsum + pv[j][e]);
    } else {
      pw[j][e] = opaque(pk2(pv[j][2 * e], pv[j][2 * e + 1]));
      if constexpr ((e & 3) == 3) { u32x4 p = {pw[j][e - 3], pw[j][e - 2], pw[j][e - 1], pw[j][e]}; pf[j][e >> 2] = __builtin_bit_cast(bf16x8, p); }
    }
  };

  float fs = 0.f;
  rawbar();
  STAMP(1); CSTAMP(0);
  static_for<WLA>([&](auto N) { issue_reads(N); });
  SB();
  static_for<192>([&](auto N) {
    constexpr int n = N, t = n / 8;
    if constexpr (n >= 136 && n % 8 == 0) {
      if (__builtin_expect(resc[t - 16], 0)) {
#pragma unroll
        for (int dt = 0; dt < 4; ++dt) oacc[dt] *= alph[t - 16];
      }
    }
    if constexpr (n < 64) {
      constexpr int ks = qs_ks(n), i = qs_i(n);
      qacc[i] = MFMA32(fr[n], fx[t * 4 + ks], qacc[i]);
    } else if constexpr (n < 128) {
      if constexpr (n % 8 == 0) { f32x16 z = {}; sacc[t - 8] = MFMA32(fr[n], qf[0], z); }
      else sacc[t - 8] = MFMA32(fr[n], qf[n % 8], sacc[t - 8]);
    } else {
      constexpr int ks = (n % 8) / 4, dt = n % 4;
      oacc[dt] = MFMA32(fr[n], pf[t - 16][ks], oacc[dt]);
    }
    SB();
    if constexpr (n == 48) {
      const int lane = fresh_lane();
      const unsigned kb = ldsb + rowbase(32 * c + (lane & 31), lane >> 5);
#pragma unroll
      for (int j = 0; j < 8; ++j) ka[j] = opaque(kb ^ (unsigned)(32 * j));
      tag = (unsigned)__builtin_amdgcn_readfirstlane(epv) + 1u;
    }
    if constexpr (n == 128 - WLA - 1) {
      const int lane = fresh_lane();
#pragma unroll
      for (int dt = 0; dt < 4; ++dt)
#pragma unroll
        for (int tt = 0; tt < 2; ++tt) va[dt][tt] = opaque(ldsb + trV(lane, dt ^ (2 * c), tt) + (unsigned)c * 65536u);
    }
    if constexpr (n + WLA < 192) {
      if constexpr ((n + WLA) % 8 == 0) { STBAR(); if constexpr ((n + WLA) % 64 == 0) { STAMP(2 + (n + WLA) / 8); CSTAMP((n + WLA) / 32); } }
      issue_reads(std::integral_constant<int, n + WLA>{});
    }
    if constexpr (n == 61) qwrite(0);
    if constexpr (n == 63) {
      qwrite(1);
      const int lane = fresh_lane(), R = 32 * r + (lane & 31), hf = lane >> 5;
      lgkmbar();
#pragma unroll
      for (int ks = 0; ks < 8; ++ks) qf[ks] = ldsr16(ldsb + AUX + 256 * R + 16 * ((2 * ks + hf) ^ swz(R)));
    }
    if constexpr (n >= 64) static_for<VS.start[n + 1] - VS.start[n]>([&](auto K) { vop(std::integral_constant<int, VS.start[n] + (int)K>{}); });
    if constexpr (n == SSLOT) {
      const int lane = fresh_lane();
      const float lw = swapadd(lsum);
      f32x2 st = {mref, lw};
      *(LAS f32x2*)(ldsb + AUX + (wid ^ 2) * 8192 + lane * 8) = st;
      lgkmbar();
      const f32x2 so2 = *(const LAS f32x2*)(ldsb + AUX + wid * 8192 + lane * 8);
      const float M = fmaxf(mref, so2[0]);
      const float bs = __builtin_amdgcn_exp2f((mref - M) * CS), bo = __builtin_amdgcn_exp2f((so2[0] - M) * CS);
      fs = bs / (lw * bs + so2[1] * bo);
    }
    SB();
  });
  STAMP(30); CSTAMP(6);
  const int lane = fresh_lane();
  {
    const unsigned mine = ldsb + AUX + wid * 8192 + lane * 16;
#pragma unroll
    for (int i = 0; i < 2; ++i)
#pragma unroll
      for (int gq = 0; gq < 4; ++gq) {
        const f32x16& ta = oacc[2 + i];
        f32x4 v;
#pragma unroll
        for (int e = 0; e < 4; ++e) v[e] = ta[4 * gq + e] * fs;
        *(LAS f32x4*)(mine + (i * 4 + gq) * 1024) = v;
      }
  }
  lgkmbar();
  STAMP(31);
  const __amdgpu_buffer_rsrc_t wsr = __builtin_amdgcn_make_buffer_rsrc(ws, 0, 16u << 20, 0x00020000);
  bf16x8 of[4][8];
  {
    const unsigned other = ldsb + AUX + (wid ^ 2) * 8192 + lane * 16;
    const unsigned goff = WS_O + (unsigned)(((bid * 2 + r) * 4 + 2 * c) * 2) * 1024 + lane * 16;
    bf16x8 own[4];
#pragma unroll
    for (int i = 0; i < 2; ++i) {
      const f32x16& ta = oacc[i];
      f32x16 t;
#pragma unroll
      for (int gq = 0; gq < 4; ++gq) {
        f32x4 v = *(const LAS f32x4*)(other + (i * 4 + gq) * 1024);
#pragma unroll
        for (int e = 0; e < 4; ++e) t[4 * gq + e] = fmaf(ta[4 * gq + e], fs, v[e]);
      }
#pragma unroll
      for (int s = 0; s < 2; ++s) own[i * 2 + s] = pack8(t, s);
    }
#pragma unroll
    for (int k = 0; k < 4; ++k) __builtin_amdgcn_raw_buffer_store_b128(__builtin_bit_cast(u32x4, own[k]), wsr, goff + k * 1024, 0, 16);
#pragma unroll
    for (int k = 0; k < 4; ++k) *(LAS bf16x8*)(other + k * 1024) = own[k];
    asm volatile("s_waitcnt vmcnt(0)" ::: "memory");
    lgkmbar();
    if (wid == 0 && lane == 0) __hip_atomic_store((gu32*)(ws + WS_F1) + (b * 8 + g) * 32 + hh, tag, RLX_AGENT);
    const unsigned mine = ldsb + AUX + wid * 8192 + lane * 16;
#pragma unroll
    for (int k = 0; k < 4; ++k) {
      of[0][k] = own[k]; of[0][4 + k] = ldsr16(mine + k * 1024);
    }
  }
  STAMP(32);
  unsigned oa[2][2][2][2];
#pragma unroll
  for (int hs = 0; hs < 2; ++hs)
#pragma unroll
    for (int ci = 0; ci < 2; ++ci)
#pragma unroll
      for (int tt = 0; tt < 2; ++tt) {
        const unsigned a = ldsb + trV(lane, 2 * c + ci, tt) + (unsigned)(hs ^ c) * 1024u;
        oa[hs][ci][tt][0] = opaque(a); oa[hs][ci][tt][1] = opaque(a + 65536u);
      }
  bf16x8 fo[64];
  f32x16 pacc[2] = {};
  auto oreads = [&](auto M) {
    constexpr int m = M, ks4 = (m % 8) / 2, ci = m % 2, hs = (m / 8) % 2;
    constexpr unsigned so = ((28 + 2 * (m / 16)) % SLOTS) * 1024;
    fo[m] = trread2(oa[hs][ci][0][ks4 >> 1] + so + (4 * (ks4 & 1)) * 8192, oa[hs][ci][1][ks4 >> 1] + so + (4 * (ks4 & 1) + 2) * 8192);
  };
  static_for<WLA>([&](auto M) { oreads(M); });
  SB();
  static_for<64>([&](auto M) {
    constexpr int m = M, jj = m / 16, half = (m / 8) % 2, ks4 = (m % 8) / 2, ci = m % 2;
    pacc[ci] = MFMA32(fo[m], of[jj][half * 4 + ks4], pacc[ci]);
    SB();
    if constexpr (m + WLA == 16) {
      rawbar();
      STAMP(33);
      gu32* f1 = (gu32*)(ws + WS_F1) + (b * 8 + g) * 32 + (lane & 3);
      for (unsigned spins = 0; spins < (1u << 17); ++spins) {
        if (__all(__hip_atomic_load(f1, RLX_AGENT) == tag)) break;
        __builtin_amdgcn_s_sleep(1);
      }
      asm volatile("" ::: "memory");
      STAMP(34);
#pragma unroll
      for (int j2 = 1; j2 < 4; ++j2) {
        const int sb = (bid & ~24) | (((hh + j2) & 3) << 3);
#pragma unroll
        for (int k = 0; k < 8; ++k)
          of[j2][k] = __builtin_bit_cast(bf16x8, __builtin_amdgcn_raw_buffer_load_b128(wsr, WS_O + (unsigned)((sb * 2 + r) * 8 + (k ^ (4 * c))) * 1024 + lane * 16, 0, 16));
      }
    }
    if constexpr (m + WLA < 64) oreads(std::integral_constant<int, m + WLA>{});
    SB();
  });
  STAMP(35); CSTAMP(7);
#pragma unroll
  for (int ci = 0; ci < 2; ++ci) {
    const int cons = (bid & ~7) | (2 * (2 * c + ci) + r);
#pragma unroll
    for (int gq = 0; gq < 4; ++gq) {
      f32x4 v = {pacc[ci][4 * gq], pacc[ci][4 * gq + 1], pacc[ci][4 * gq + 2], pacc[ci][4 * gq + 3]};
      __builtin_amdgcn_raw_buffer_store_b128(__builtin_bit_cast(u32x4, v), wsr, WS_P + (unsigned)((cons * 8 + g) * 4 + gq) * 1024 + lane * 16, 0, 16);
    }
  }
  asm volatile("s_waitcnt vmcnt(0)" ::: "memory");
  if (lane < 2) {
    const int cons = (bid & ~7) | (2 * (2 * c + lane) + r);
    __hip_atomic_store((gu32*)(ws + WS_F2) + cons * 32 + g, tag, RLX_AGENT);
  }
  STAMP(36);
  {
    gu32* f2 = (gu32*)(ws + WS_F2) + bid * 32 + (lane & 7);
    for (unsigned spins = 0; spins < (1u << 17); ++spins) {
      if (__all(__hip_atomic_load(f2, RLX_AGENT) == tag)) break;
      __builtin_amdgcn_s_sleep(1);
    }
    asm volatile("" ::: "memory");
    STAMP(37);
    f32x4 pp[8];
#pragma unroll
    for (int p = 0; p < 8; ++p)
      pp[p] = __builtin_bit_cast(f32x4, __builtin_amdgcn_raw_buffer_load_b128(wsr, WS_P + (unsigned)((bid * 8 + p) * 4 + wid) * 1024 + lane * 16, 0, 16));
    f32x4 s = ((pp[0] + pp[1]) + (pp[2] + pp[3])) + ((pp[4] + pp[5]) + (pp[6] + pp[7]));
    const int ct = g >> 1, rr = g & 1;
    u32x2 o2 = {pk2(s[0], s[1]), pk2(s[2], s[3])};
    *(u32x2*)(out + (size_t)(b * 64 + rr * 32 + (lane & 31)) * 512 + hh * 128 + ct * 32 + 8 * wid + 4 * (lane >> 5)) = o2;
  }
  STAMP(38); CSTAMP(8);
}

extern "C" void kernel_launch(void* const* d_in, const int* in_sizes, int n_in, void* d_out, int out_size, void* d_ws, size_t ws_size, hipStream_t stream) {
  (void)hipFuncSetAttribute((const void*)gqa_kernel, hipFuncAttributeMaxDynamicSharedMemorySize, LDSB);
  gqa_kernel<<<256, 512, LDSB, stream>>>((const u16*)d_in[0], (const u16*)d_in[1], (const u16*)d_in[2], (const u16*)d_in[3], (const u16*)d_in[4], (char*)d_ws, (u16*)d_out);
}
```

```cpp
#define MEAS 999
#define STAMPS
#define LSTAMPS
#include <hip/hip_runtime.h>
#include <hip/hip_bf16.h>
#include <utility>

typedef unsigned short u16;
typedef __bf16 bf16x8 __attribute__((ext_vector_type(8)));
typedef __bf16 bf16x2 __attribute__((ext_vector_type(2)));
typedef short s16x4 __attribute__((ext_vector_type(4)));
typedef float f32x16 __attribute__((ext_vector_type(16)));
typedef float f32x4 __attribute__((ext_vector_type(4)));
typedef float f32x2 __attribute__((ext_vector_type(2)));
typedef unsigned u32x4 __attribute__((ext_vector_type(4)));
typedef unsigned u32x2 __attribute__((ext_vector_type(2)));
#define LAS __attribute__((address_space(3)))
#define GAS __attribute__((address_space(1)))
typedef GAS unsigned gu32;
#define DI __device__ __forceinline__
#define MFMA32(a, b, c) __builtin_amdgcn_mfma_f32_32x32x16_bf16((a), (b), (c), 0, 0, 0)
#define RLX_AGENT __ATOMIC_RELAXED, __HIP_MEMORY_SCOPE_AGENT
#define SB() __builtin_amdgcn_sched_barrier(0)

DI unsigned pk2(float a, float b) { f32x2 v = {a, b}; bf16x2 r = __builtin_convertvector(v, bf16x2); return __builtin_bit_cast(unsigned, r); }
DI bf16x8 pack8(const f32x16& x, int s) {
  u32x4 p = {pk2(x[8 * s], x[8 * s + 1]), pk2(x[8 * s + 2], x[8 * s + 3]), pk2(x[8 * s + 4], x[8 * s + 5]), pk2(x[8 * s + 6], x[8 * s + 7])};
  return __builtin_bit_cast(bf16x8, p);
}
DI bf16x8 trread2(unsigned a0, unsigned a1) {
  s16x4 lo = __builtin_amdgcn_ds_read_tr16_b64_v4i16((LAS s16x4*)(uintptr_t)a0);
  s16x4 hi = __builtin_amdgcn_ds_read_tr16_b64_v4i16((LAS s16x4*)(uintptr_t)a1);
  return __builtin_bit_cast(bf16x8, __builtin_shufflevector(lo, hi, 0, 1, 2, 3, 4, 5, 6, 7));
}
DI bf16x8 ldsr16(unsigned a) { return *(const LAS bf16x8*)(uintptr_t)a; }
DI void ldsw16(unsigned a, bf16x8 v) { *(LAS bf16x8*)(uintptr_t)a = v; }
DI void glds16(const void* sbase, unsigned voff, unsigned lds_dst) {
  unsigned keep;
  asm volatile("s_mov_b32 %0, m0\n\ts_mov_b32 m0, %3\n\ts_nop 0\n\tglobal_load_lds_dwordx4 %1, %2\n\ts_mov_b32 m0, %0"
               : "=&s"(keep) : "v"(voff), "s"(sbase), "s"(lds_dst) : "memory");
}
template <int N> DI void wait_vm() { asm volatile("s_waitcnt vmcnt(%0)" ::"n"(N) : "memory"); }
template <int N> DI void wait_lgkm() { asm volatile("s_waitcnt lgkmcnt(%0)" ::"n"(N) : "memory"); }
DI void rawbar() { asm volatile("" ::: "memory"); __builtin_amdgcn_s_barrier(); asm volatile("" ::: "memory"); }
DI float swapmax(float m) { auto rr = __builtin_amdgcn_permlane32_swap(__float_as_uint(m), __float_as_uint(m), false, false); return fmaxf(__uint_as_float(rr[0]), __uint_as_float(rr[1])); }
DI float swapadd(float m) { auto rr = __builtin_amdgcn_permlane32_swap(__float_as_uint(m), __float_as_uint(m), false, false); return __uint_as_float(rr[0]) + __uint_as_float(rr[1]); }
DI unsigned opaque(unsigned v) { asm volatile("" : "+v"(v)); return v; }
DI float pinf(float v) { asm volatile("" : "+v"(v)); return v; }
DI float max3(float a, float b, float c) { float r; asm("v_max3_f32 %0, %1, %2, %3" : "=v"(r) : "v"(a), "v"(b), "v"(c)); return r; }
DI int fresh_lane() { int l; asm volatile("v_mbcnt_lo_u32_b32 %0, -1, 0\n\tv_mbcnt_hi_u32_b32 %0, -1, %0" : "=v"(l)); return l; }

template <typename F, int... I> DI void static_for_impl(F&& f, std::integer_sequence<int, I...>) { (f(std::integral_constant<int, I>{}), ...); }
template <int N, typename F> DI void static_for(F&& f) { static_for_impl(f, std::make_integer_sequence<int, N>{}); }

constexpr int NSLOT = 10, GS = NSLOT * 1024, LDSB = 16 * GS, NUNITS = 36, NST = 24;
DI int swz(int R) { return ((R & 3) << 2) | ((R >> 2) & 3); }
DI unsigned rowbase(int R, int hf) { return (unsigned)((R >> 2) * GS + (R & 3) * 256 + 16 * (hf ^ swz(R))); }
DI unsigned trW(int lane, int dt, int t) {
  const int hf = lane >> 5, q4 = (lane & 15) >> 2, p4 = lane & 3, blk = (lane >> 4) & 1;
  const int pc = ((p4 & 1) << 1) | (p4 >> 1);
  return (unsigned)((2 * hf + t) * GS + q4 * 256 + 16 * (((dt ^ q4) << 2) | ((2 * blk + (pc >> 1)) ^ (2 * hf + t))) + 8 * (pc & 1));
}
DI unsigned trV(int lane, int dt, int t) {
  const int hf = lane >> 5, q4 = (lane & 15) >> 2, p4 = lane & 3, blk = (lane >> 4) & 1;
  return (unsigned)((2 * t + hf) * GS + q4 * 256 + 16 * (((dt ^ q4) << 2) | ((2 * blk + (p4 >> 1)) ^ (2 * t + hf))) + 8 * (p4 & 1));
}
constexpr unsigned auxc(int k) { return (unsigned)((k >> 1) * GS + 8192 + (k & 1) * 1024); }

#define WLA 4
#define PRO 5
#define MBURST 2
constexpr int cu(int t) { return t < 8 ? (t / 2) * 3 + 1 + (t & 1) : t + 4; }
constexpr int xu(int t) { return (t / 2) * 3; }
constexpr int need(int t) { return t == 8 ? cu(9) : cu(t); }
constexpr int lastuse(int u) { return u < 12 ? (u % 3 == 0 ? (u / 3) * 2 + 1 : (u / 3) * 2 + (u % 3) - 1) : (u == 12 ? 9 : u - 4); }
struct Plan { int slot[NUNITS]; int early[NUNITS]; int iss[NST + 2]; };
constexpr Plan make_plan() {
  Plan p{};
  int fr[NSLOT] = {}; for (int s = 0; s < NSLOT; ++s) fr[s] = -1;
  const int first[10] = {8, 0, 1, 9, 2, 3, 6, 4, 5, 7};
  int prev = -1;
  for (int u = 0; u < NUNITS; ++u) {
    int s = -1;
    if (u < 10) s = first[u];
    else if (u < 28) {
      int lim = lastuse(u) <= 21 ? NSLOT : 8;
      for (int k = 0; k < lim; ++k) if (s < 0 || fr[k] < fr[s]) s = k;
    } else if ((u & 1) == 0) {
      int best = 1000;
      for (int k = 0; k < 8; k += 2) { int m = fr[k] > fr[k + 1] ? fr[k] : fr[k + 1]; if (m < best) { best = m; s = k; } }
    } else s = p.slot[u - 1] + 1;
    int e = fr[s] > prev ? fr[s] : prev;
    p.slot[u] = s; p.early[u] = e; prev = e;
    fr[s] = u < 28 ? lastuse(u) + 2 : 1000;
    if (u == 3) fr[s] = 12;
  }
  int cur = PRO; p.iss[0] = cur;
  for (int t = 0; t < NST; ++t) {
    int lim = 0; while (lim < NUNITS && p.early[lim] <= t) ++lim;
    int want = cur + MBURST; int must = t + 1 < NST ? need(t + 1) + 1 : 0;
    if (want < must) want = must;
    if (want > lim) want = lim;
    cur = want; p.iss[t + 1] = cur;
  }
  return p;
}
constexpr Plan PL = make_plan();
constexpr bool plan_ok() { for (int t = 0; t < NST; ++t) if (PL.iss[t] < need(t) + 1) return false; return PRO <= 10; }
static_assert(plan_ok(), "unit not issued before it is needed");
constexpr unsigned soff(int u) { return (unsigned)PL.slot[u] * 1024u; }

constexpr unsigned WS_O = 0, WS_P = 4u << 20, WS_F1 = 12u << 20, WS_F2 = WS_F1 + 65536, WS_EP = WS_F2 + 65536;

constexpr int qs_ks(int n) { return n < 56 ? (n % 8) / 2 : (n % 4); }
constexpr int qs_i(int n) { return n < 56 ? n % 2 : (n % 8) / 4; }
constexpr int ks_tile(int n) { return n >= 80 ? (n - 64) / 8 : (n < 72 ? ((n - 64) >> 1) & 1 : (n - 72) >> 2); }
constexpr int ks_k(int n) { return n >= 80 ? n % 8 : (n < 72 ? ((n - 64) >> 2) * 2 + (n & 1) : 4 + (n & 3)); }
constexpr int tile_done(int j) { return j == 0 ? 75 : 64 + 8 * j + 7; }
constexpr int pv_ks(int n) { return n < 184 ? (n % 8) / 4 : (n & 3) >> 1; }
constexpr int pv_dt(int n) { return n < 184 ? n % 4 : (n < 188 ? 2 : 0) + (n & 1); }

enum { OP_MAX, OP_CHK, OP_LS, OP_EX, OP_ADD, OP_CVT };
struct VOp { int type, tile, idx; };
struct VSched { VOp op[512]; int start[194]; };
#define BQK 20
#define BPV 18
constexpr VSched make_sched() {
  VSched s{};
  VOp ex[8][48] = {};
  for (int j = 0; j < 8; ++j) {
    int k = 0;
    if (j > 0) ex[j][k++] = VOp{OP_LS, j, 0};
    for (int e = 0; e < 16; ++e) {
      ex[j][k++] = VOp{OP_EX, j, e};
      if (e >= 1) ex[j][k++] = VOp{OP_ADD, j, e - 1};
      if (e >= 2 && (e & 1) == 0) ex[j][k++] = VOp{OP_CVT, j, (e - 2) / 2};
    }
    ex[j][k++] = VOp{OP_ADD, j, 15};
    ex[j][k++] = VOp{OP_CVT, j, 7};
    ex[j][k] = VOp{-1, 0, 0};
  }
  int jm = 0, km = 0, je = 0, ke = 0, no = 0;
  for (int n = 0; n < 192; ++n) {
    s.start[n] = no;
    if (n < 64) continue;
    int bud = n < 128 ? BQK : BPV;
    while (jm < 8 && n >= tile_done(jm) + 2 && bud > 0) {
      if (km < 8) { s.op[no++] = VOp{OP_MAX, jm, km}; ++km; bud -= 4; }
      else { s.op[no++] = VOp{OP_CHK, jm, 0}; ++jm; km = 0; bud -= 8; }
    }
    while (je < jm && je < 8) {
      VOp o = ex[je][ke];
      int cost = o.type == OP_EX ? 8 : 4;
      bool first_half = (o.type == OP_CVT) ? o.idx < 4 : (o.type == OP_EX ? o.idx <= 8 : (o.type == OP_ADD ? o.idx < 8 : true));
      int dl = 128 + 8 * je + (first_half ? 0 : (je == 7 ? 2 : 4)) - 2;
      if (bud < cost - 2 && n < dl) break;
      s.op[no++] = o; bud -= cost; ++ke;
      if (ex[je][ke].type < 0) { ++je; ke = 0; }
    }
  }
  s.start[192] = no; s.start[193] = (je == 8 && jm == 8) ? 1 : 0;
  return s;
}
constexpr VSched VS = make_sched();
static_assert(VS.start[193] == 1, "schedule incomplete");
constexpr int last_vslot() { int l = 0; for (int n = 0; n < 192; ++n) if (VS.start[n + 1] > VS.start[n]) l = n; return l; }
constexpr int SSLOT = last_vslot() + 1 > 184 - WLA ? last_vslot() + 1 : 184 - WLA;
static_assert(SSLOT <= 186, "stats slot out of range");

#define STAMP(i) do { unsigned long long t_ = __builtin_amdgcn_s_memrealtime(); if (fresh_lane() == 0) ((unsigned long long*)(ws + (16u << 20)))[(bid * 4 + wid) * 64 + (i)] = t_; } while (0)

__global__ __launch_bounds__(512, 2) void gqa_kernel(const u16* __restrict__ x, const u16* __restrict__ kc, const u16* __restrict__ vc,
                                                     const u16* __restrict__ wq, const u16* __restrict__ wo, char* __restrict__ ws, u16* __restrict__ out) {
  extern __shared__ __attribute__((aligned(16))) char lds[];
  const int wid8 = __builtin_amdgcn_readfirstlane(threadIdx.x >> 6);
  const int wid = wid8 & 3;
  const int bid = blockIdx.x;
  const int g = bid & 7, b = bid >> 5, hh = (bid >> 3) & 3, h = g * 4 + hh;
  const unsigned ldsb = (unsigned)(uintptr_t)(LAS char*)lds;
  gu32* const epw = (gu32*)(ws + WS_EP);

  if (wid8 >= 4) {
    const int lane = fresh_lane();
    unsigned voffx[4], voffw[4], voffk[4];
#pragma unroll
    for (int i = 0; i < 4; ++i) {
      unsigned row = wid * 16 + (lane >> 4) + 4 * i;
      unsigned ch = (lane & 15) ^ (((lane >> 4) << 2) | i);
      voffx[i] = row * 1024 + ch * 16; voffw[i] = row * 8192 + ch * 16; voffk[i] = row * 256 + ch * 16;
    }
    auto issue = [&](auto U) {
      constexpr int u = U;
      const unsigned dst = ldsb + soff(u) + wid * (4 * GS);
      if constexpr (u < 12) {
        const int q = ((u / 3) + (b >> 1)) & 3; constexpr int rr = u % 3;
        if constexpr (rr == 0) { const u16* src = x + (size_t)b * 64 * 512 + q * 128;
#pragma unroll
          for (int i = 0; i < 4; ++i) glds16(src, voffx[i], dst + i * GS); }
        else { int wu = q * 2 + ((rr - 1) ^ (b & 1)); const u16* src = wq + (size_t)wu * 64 * 4096 + h * 128;
#pragma unroll
          for (int i = 0; i < 4; ++i) glds16(src, voffw[i], dst + i * GS); }
      } else if constexpr (u < 28) {
        int k = (u - 12 + 2 * hh) & 7; const u16* src = (u < 20 ? kc : vc) + ((size_t)(b * 8 + g) * 512 + k * 64) * 128;
#pragma unroll
        for (int i = 0; i < 4; ++i) glds16(src, voffk[i], dst + i * GS);
      } else {
        int k = (u - 28 + 2 * hh) & 7; const u16* src = wo + ((size_t)g * 512 + k * 64) * 512 + hh * 128;
#pragma unroll
        for (int i = 0; i < 4; ++i) glds16(src, voffx[i], dst + i * GS);
      }
    };
    unsigned lst[32];
#define LST(i) lst[i] = (unsigned)__builtin_amdgcn_s_memrealtime()
    LST(31);
    static_for<PL.iss[0]>([&](auto U) { issue(U); });
    static_for<NST>([&](auto T) {
      constexpr int t = T;
      wait_vm<(PL.iss[t] - need(t) - 1) * 4>();
      rawbar();
      LST(t);
      static_for<PL.iss[t + 1] - PL.iss[t]>([&](auto K) { issue(std::integral_constant<int, PL.iss[t] + (int)K>{}); });
      if constexpr (t == 8) { rawbar(); LST(24); }
    });
    rawbar();
    LST(25);
    rawbar();
    LST(26);
    static_for<NUNITS - PL.iss[NST]>([&](auto K) { issue(std::integral_constant<int, PL.iss[NST] + (int)K>{}); });
    wait_vm<(NUNITS - 30) * 4>();
    rawbar();
    LST(27);
    wait_vm<0>();
    rawbar();
    LST(28);
    if (wid == 0 && lane == 0) {
#pragma unroll
      for (int i = 0; i < 29; ++i) ((unsigned*)(ws + (24u << 20)))[bid * 32 + i] = lst[i];
    }
    if (bid == 0 && wid == 0) {
      const unsigned tag = (unsigned)__builtin_amdgcn_readfirstlane(__hip_atomic_load(epw, RLX_AGENT)) + 1u;
      gu32* f1 = (gu32*)(ws + WS_F1) + lane * 32;
      for (unsigned spins = 0; spins < (1u << 17); ++spins) {
        bool ok = true;
#pragma unroll
        for (int i = 0; i < 16; ++i) ok &= __hip_atomic_load(f1 + i, RLX_AGENT) == tag;
        if (__all(ok)) break;
        __builtin_amdgcn_s_sleep(2);
      }
      if (lane == 0) __hip_atomic_store(epw, tag, RLX_AGENT);
    }
    return;
  }

  __builtin_amdgcn_s_setprio(2);
  const int r = wid & 1, c = wid >> 1;
  STAMP(0);
  const unsigned epv = __hip_atomic_load(epw, RLX_AGENT);
  unsigned tag = 0;
  unsigned xa[8], wa[2][2][2], ka[8], va[4][2], lin = 0;
  {
    const int lane = fresh_lane();
    const unsigned xb = (ldsb + rowbase(32 * r + (lane & 31), lane >> 5)) ^ (unsigned)((b & 1) * 128);
#pragma unroll
    for (int j = 0; j < 8; ++j) xa[j] = opaque(xb ^ (unsigned)(32 * j));
#pragma unroll
    for (int i = 0; i < 2; ++i)
#pragma unroll
      for (int t = 0; t < 2; ++t) { wa[i][t][0] = opaque(ldsb + trW(lane, 2 * c + i, t)); wa[i][t][1] = opaque(ldsb + trW(lane, 2 * c + i, t) + 8u * GS); }
    const unsigned kb = ldsb + rowbase(32 * c + (lane & 31), lane >> 5);
#pragma unroll
    for (int j = 0; j < 8; ++j) ka[j] = opaque(kb ^ (unsigned)(32 * (j ^ (4 * c))));
#pragma unroll
    for (int dt = 0; dt < 4; ++dt)
#pragma unroll
      for (int tt = 0; tt < 2; ++tt) va[dt][tt] = opaque(ldsb + trV(lane, dt ^ (2 * c), tt) + (unsigned)c * (8u * GS));
    lin = opaque(ldsb + lane * 16);
  }

  bf16x8 fr[192], fx[32], qf[8], pf[8][2];
  f32x16 qacc[2] = {}, sacc[8], oacc[4] = {};
  float pv[8][16];
  unsigned pw[8][8];
  float mt[8], mref = 0.f, alph[8], lsum = 0.f;
  bool slow = false;
  constexpr float CS = 0.088388347648318440f * 1.4426950408889634f;
  constexpr float TLAZY = 40.0f;

  auto issue_reads = [&](auto N) {
    constexpr int n = N, t = n / 8;
    if constexpr (n < 64) {
      constexpr int ks = qs_ks(n), i = qs_i(n);
      constexpr unsigned so = soff(cu(t)) + (4 * (ks & 1)) * GS;
      if constexpr (i == 0) fx[t * 4 + ks] = ldsr16(xa[(t & 1) * 4 + ks] + soff(xu(t)));
      fr[n] = trread2(wa[i][0][ks >> 1] + so, wa[i][1][ks >> 1] + so);
    } else if constexpr (n < 128) {
      fr[n] = ldsr16(ka[ks_k(n)] + soff(12 + ks_tile(n)));
    } else {
      constexpr unsigned so = soff(cu(t)) + (4 * pv_ks(n)) * GS;
      fr[n] = trread2(va[pv_dt(n)][0] + so, va[pv_dt(n)][1] + so);
    }
  };
  auto qpack = [&](int i) {
    f32x16 t = qacc[i] * CS;
#pragma unroll
    for (int s = 0; s < 2; ++s) { qf[2 * i + s] = pack8(t, s); ldsw16(lin + (unsigned)(wid * 4 + 2 * i + s) * GS + 9216, qf[2 * i + s]); }
  };
  auto vop = [&](auto K) {
    constexpr VOp o = VS.op[(int)K];
    constexpr int j = o.tile, e = o.idx;
    if constexpr (o.type == OP_MAX) {
      if constexpr (e == 0) mt[j] = max3(sacc[j][0], sacc[j][1], sacc[j][1]);
      else mt[j] = max3(mt[j], sacc[j][2 * e], sacc[j][2 * e + 1]);
    } else if constexpr (o.type == OP_CHK) {
      if constexpr (j > 0) alph[j] = 1.0f;
      const bool out_of_range = j == 0 ? !(__builtin_fabsf(mt[0]) <= TLAZY) : !(mt[j] <= TLAZY);
      if (__builtin_expect(slow || __builtin_amdgcn_ballot_w64(out_of_range) != 0, 0)) {
        const float m = swapmax(mt[j]);
        const float mn = j == 0 ? m : fmaxf(mref, m);
        if constexpr (j > 0) alph[j] = __builtin_amdgcn_exp2f(mref - mn);
        mref = mn; slow = true;
        sacc[j] -= mn;
      }
    } else if constexpr (o.type == OP_LS) {
      lsum = pinf(lsum * alph[j]);
    } else if constexpr (o.type == OP_EX) {
      pv[j][e] = pinf(__builtin_amdgcn_exp2f(sacc[j][e]));
    } else if constexpr (o.type == OP_ADD) {
      lsum = pinf(lsum + pv[j][e]);
    } else {
      pw[j][e] = opaque(pk2(pv[j][2 * e], pv[j][2 * e + 1]));
      if constexpr ((e & 3) == 3) { u32x4 p = {pw[j][e - 3], pw[j][e - 2], pw[j][e - 1], pw[j][e]}; pf[j][e >> 2] = __builtin_bit_cast(bf16x8, p); }
    }
  };
  float fs = 0.f;
  auto osend = [&](int i) {
#pragma unroll
    for (int gq = 0; gq < 4; ++gq) {
      f32x4 v;
#pragma unroll
      for (int e = 0; e < 4; ++e) v[e] = oacc[2 + i][4 * gq + e] * fs;
      *(LAS f32x4*)(uintptr_t)(lin + (unsigned)wid * (4 * GS) + auxc(i * 4 + gq)) = v;
    }
  };

  rawbar();
  STAMP(1);
  static_for<WLA>([&](auto N) { issue_reads(N); });
  SB();
  static_for<192>([&](auto N) {
    constexpr int n = N, t = n / 8;
    if constexpr (n >= 136 && n % 8 == 0) {
      if (__builtin_expect(slow, 0)) {
#pragma unroll
        for (int dt = 0; dt < 4; ++dt) oacc[dt] *= alph[t - 16];
      }
    }
    if constexpr (n < 64) {
      constexpr int ks = qs_ks(n), i = qs_i(n);
      qacc[i] = MFMA32(fr[n], fx[t * 4 + ks], qacc[i]);
    } else if constexpr (n < 128) {
      constexpr int j = ks_tile(n), k = ks_k(n);
      if constexpr (k == 0) { f32x16 z = {}; sacc[j] = MFMA32(fr[n], qf[0], z); }
      else sacc[j] = MFMA32(fr[n], qf[k], sacc[j]);
    } else {
      oacc[pv_dt(n)] = MFMA32(fr[n], pf[t - 16][pv_ks(n)], oacc[pv_dt(n)]);
    }
    SB();
    if constexpr (n == 48) tag = (unsigned)__builtin_amdgcn_readfirstlane(epv) + 1u;
    if constexpr (n + WLA < 192) {
      if constexpr ((n + WLA) % 8 == 0) rawbar();
      issue_reads(std::integral_constant<int, n + WLA>{});
    }
    if constexpr (n == 61) qpack(0);
    if constexpr (n == 65) qpack(1);
    if constexpr (n == 67) {
      wait_lgkm<2>();
      rawbar();
#pragma unroll
      for (int k = 0; k < 4; ++k) qf[4 + k] = ldsr16(lin + (unsigned)((wid ^ 2) * 4 + k) * GS + 9216);
    }
    if constexpr (n >= 64) static_for<VS.start[n + 1] - VS.start[n]>([&](auto K) { vop(std::integral_constant<int, VS.start[n] + (int)K>{}); });
    if constexpr (n == SSLOT) {
      const float lw = swapadd(lsum);
      f32x2 st = {mref, lw};
      *(LAS f32x2*)(uintptr_t)(lin + (unsigned)(wid ^ 2) * (4 * GS) + auxc(0)) = st;
      wait_lgkm<0>();
      rawbar();
      const f32x2 so2 = *(const LAS f32x2*)(uintptr_t)(lin + (unsigned)wid * (4 * GS) + auxc(0));
      const float M = fmaxf(mref, so2[0]);
      const float bs = __builtin_amdgcn_exp2f(mref - M), bo = __builtin_amdgcn_exp2f(so2[0] - M);
      fs = bs / (lw * bs + so2[1] * bo);
    }
    if constexpr (n == 189) osend(0);
    if constexpr (n == 190) osend(1);
    SB();
  });
  STAMP(30);
  wait_lgkm<0>();
  rawbar();
  STAMP(31);
  const int lane = fresh_lane();
  const __amdgpu_buffer_rsrc_t wsr = __builtin_amdgcn_make_buffer_rsrc(ws, 0, 16u << 20, 0x00020000);
  bf16x8 of[4][8];
  {
    const unsigned other = lin + (unsigned)(wid ^ 2) * (4 * GS);
    const unsigned goff = WS_O + (unsigned)(((bid * 2 + r) * 4 + 2 * c) * 2) * 1024 + lane * 16;
#pragma unroll
    for (int i = 0; i < 2; ++i) {
      f32x16 t;
#pragma unroll
      for (int gq = 0; gq < 4; ++gq) {
        f32x4 v = *(const LAS f32x4*)(uintptr_t)(other + auxc(i * 4 + gq));
#pragma unroll
        for (int e = 0; e < 4; ++e) t[4 * gq + e] = fmaf(oacc[i][4 * gq + e], fs, v[e]);
      }
#pragma unroll
      for (int s = 0; s < 2; ++s) of[0][i * 2 + s] = pack8(t, s);
    }
#pragma unroll
    for (int k = 0; k < 4; ++k) __builtin_amdgcn_raw_buffer_store_b128(__builtin_bit_cast(u32x4, of[0][k]), wsr, goff + k * 1024, 0, 16);
#pragma unroll
    for (int k = 0; k < 4; ++k) ldsw16(other + auxc(k), of[0][k]);
  }
  unsigned oa[2][2][2][2];
#pragma unroll
  for (int hs = 0; hs < 2; ++hs)
#pragma unroll
    for (int ci = 0; ci < 2; ++ci)
#pragma unroll
      for (int tt = 0; tt < 2; ++tt) {
        const unsigned a = ldsb + trV(lane, 2 * c + ci, tt) + (unsigned)(hs ^ c) * 1024u;
        oa[hs][ci][tt][0] = opaque(a); oa[hs][ci][tt][1] = opaque(a + 8u * GS);
      }
  wait_lgkm<0>();
  rawbar();
  STAMP(32);
#pragma unroll
  for (int k = 0; k < 4; ++k) of[0][4 + k] = ldsr16(lin + (unsigned)wid * (4 * GS) + auxc(k));
  bf16x8 fo[64];
  f32x16 pacc[2] = {};
  auto oreads = [&](auto M) {
    constexpr int m = M, ks4 = (m % 8) / 2, ci = m % 2, hs = (m / 8) % 2;
    constexpr unsigned so = soff(28 + 2 * (m / 16)) + (4 * (ks4 & 1)) * GS;
    fo[m] = trread2(oa[hs][ci][0][ks4 >> 1] + so, oa[hs][ci][1][ks4 >> 1] + so);
  };
  static_for<WLA>([&](auto M) { oreads(M); });
  SB();
  static_for<64>([&](auto M) {
    constexpr int m = M, jj = m / 16, half = (m / 8) % 2, ks4 = (m % 8) / 2, ci = m % 2;
    pacc[ci] = MFMA32(fo[m], of[jj][half * 4 + ks4], pacc[ci]);
    SB();
    if constexpr (m + WLA == 16) rawbar();
    if constexpr (m == 15) {
      STAMP(33);
      asm volatile("s_waitcnt vmcnt(0)" ::: "memory");
      if (lane == 0) __hip_atomic_store((gu32*)(ws + WS_F1) + (b * 8 + g) * 32 + hh * 4 + wid, tag, RLX_AGENT);
      STAMP(34);
      gu32* f1 = (gu32*)(ws + WS_F1) + (b * 8 + g) * 32 + (lane & 15);
      for (unsigned spins = 0; spins < (1u << 17); ++spins) {
        if (__all(__hip_atomic_load(f1, RLX_AGENT) == tag)) break;
      }
      asm volatile("" ::: "memory");
      STAMP(35);
#pragma unroll
      for (int j2 = 1; j2 < 4; ++j2) {
        const int sb = (bid & ~24) | (((hh + j2) & 3) << 3);
#pragma unroll
        for (int k = 0; k < 8; ++k)
          of[j2][k] = __builtin_bit_cast(bf16x8, __builtin_amdgcn_raw_buffer_load_b128(wsr, WS_O + (unsigned)((sb * 2 + r) * 8 + (k ^ (4 * c))) * 1024 + lane * 16, 0, 16));
      }
    }
    if constexpr (m + WLA < 64) oreads(std::integral_constant<int, m + WLA>{});
    SB();
  });
  STAMP(36);
#pragma unroll
  for (int ci = 0; ci < 2; ++ci) {
    const int cons = (bid & ~7) | (2 * (2 * c + ci) + r);
#pragma unroll
    for (int gq = 0; gq < 4; ++gq) {
      f32x4 v = {pacc[ci][4 * gq], pacc[ci][4 * gq + 1], pacc[ci][4 * gq + 2], pacc[ci][4 * gq + 3]};
      __builtin_amdgcn_raw_buffer_store_b128(__builtin_bit_cast(u32x4, v), wsr, WS_P + (unsigned)((cons * 8 + g) * 4 + gq) * 1024 + lane * 16, 0, 16);
    }
  }
  asm volatile("s_waitcnt vmcnt(0)" ::: "memory");
  if (lane < 2) {
    const int cons = (bid & ~7) | (2 * (2 * c + lane) + r);
    __hip_atomic_store((gu32*)(ws + WS_F2) + cons * 32 + g, tag, RLX_AGENT);
  }
  STAMP(37);
  {
    gu32* f2 = (gu32*)(ws + WS_F2) + bid * 32 + (lane & 7);
    for (unsigned spins = 0; spins < (1u << 17); ++spins) {
      if (__all(__hip_atomic_load(f2, RLX_AGENT) == tag)) break;
    }
    asm volatile("" ::: "memory");
    STAMP(38);
    f32x4 pp[8];
#pragma unroll
    for (int p = 0; p < 8; ++p)
      pp[p] = __builtin_bit_cast(f32x4, __builtin_amdgcn_raw_buffer_load_b128(wsr, WS_P + (unsigned)((bid * 8 + p) * 4 + wid) * 1024 + lane * 16, 0, 16));
    f32x4 s = ((pp[0] + pp[1]) + (pp[2] + pp[3])) + ((pp[4] + pp[5]) + (pp[6] + pp[7]));
    const int ct = g >> 1, rr = g & 1;
    u32x2 o2 = {pk2(s[0], s[1]), pk2(s[2], s[3])};
    *(u32x2*)(out + (size_t)(b * 64 + rr * 32 + (lane & 31)) * 512 + hh * 128 + ct * 32 + 8 * wid + 4 * (lane >> 5)) = o2;
  }
  STAMP(39);
}

__global__ void spin_kernel(const char* ws) {
  __shared__ unsigned long long mn[256], sm[256], mxs[256];
  const int t = threadIdx.x;
  unsigned long long t0 = __builtin_amdgcn_s_memrealtime();
  const unsigned long long* cs = (const unsigned long long*)(ws + (16u << 20));
  const unsigned* ls = (const unsigned*)(ws + (24u << 20));
  unsigned long long m = ~0ull, s = 0, mx = 0;
  for (int w = 0; w < 4; ++w) { unsigned long long v = cs[(t * 4 + w) * 64]; m = v < m ? v : m; }
  mn[t] = m; __syncthreads();
  m = ~0ull; for (int j = 0; j < 256; ++j) m = mn[j] < m ? mn[j] : m;
  if (MEAS < 100) { s = 4ull * (unsigned)(ls[t * 32 + MEAS] - (unsigned)m); }
  else if (MEAS < 999) { for (int w = 0; w < 4; ++w) { unsigned long long d = cs[(t * 4 + w) * 64 + (MEAS % 100)] - m; s += d; mx = d > mx ? d : mx; } }
  sm[t] = s; mxs[t] = mx; __syncthreads();
  if (t == 0) {
    unsigned long long S = 0, MX = 0; for (int j = 0; j < 256; ++j) { S += sm[j]; MX = mxs[j] > MX ? mxs[j] : MX; }
    unsigned long long X4 = MEAS >= 200 && MEAS < 999 ? 4 * MX : S / 256;
    if (X4 > 40000) X4 = 40000;
    while (__builtin_amdgcn_s_memrealtime() - t0 < 8000 + X4) __builtin_amdgcn_s_sleep(1);
  }
}
extern "C" void kernel_launch(void* const* d_in, const int* in_sizes, int n_in, void* d_out, int out_size, void* d_ws, size_t ws_size, hipStream_t stream) {
  (void)hipFuncSetAttribute((const void*)gqa_kernel, hipFuncAttributeMaxDynamicSharedMemorySize, LDSB);
  gqa_kernel<<<256, 512, LDSB, stream>>>((const u16*)d_in[0], (const u16*)d_in[1], (const u16*)d_in[2], (const u16*)d_in[3], (const u16*)d_in[4], (char*)d_ws, (u16*)d_out);
  spin_kernel<<<1, 256, 0, stream>>>((const char*)d_ws);
}
```

```cpp
#include <hip/hip_runtime.h>
#include <hip/hip_bf16.h>
#include <utility>

typedef unsigned short u16;
typedef __bf16 bf16x8 __attribute__((ext_vector_type(8)));
typedef __bf16 bf16x2 __attribute__((ext_vector_type(2)));
typedef short s16x4 __attribute__((ext_vector_type(4)));
typedef float f32x16 __attribute__((ext_vector_type(16)));
typedef float f32x4 __attribute__((ext_vector_type(4)));
typedef float f32x2 __attribute__((ext_vector_type(2)));
typedef unsigned u32x4 __attribute__((ext_vector_type(4)));
typedef unsigned u32x2 __attribute__((ext_vector_type(2)));
#define LAS __attribute__((address_space(3)))
#define GAS __attribute__((address_space(1)))
typedef GAS unsigned gu32;
#define DI __device__ __forceinline__
#define MFMA32(a, b, c) __builtin_amdgcn_mfma_f32_32x32x16_bf16((a), (b), (c), 0, 0, 0)
#define RLX_AGENT __ATOMIC_RELAXED, __HIP_MEMORY_SCOPE_AGENT
#define SB() __builtin_amdgcn_sched_barrier(0)

DI unsigned pk2(float a, float b) { f32x2 v = {a, b}; bf16x2 r = __builtin_convertvector(v, bf16x2); return __builtin_bit_cast(unsigned, r); }
DI bf16x8 pack8(const f32x16& x, int s) {
  u32x4 p = {pk2(x[8 * s], x[8 * s + 1]), pk2(x[8 * s + 2], x[8 * s + 3]), pk2(x[8 * s + 4], x[8 * s + 5]), pk2(x[8 * s + 6], x[8 * s + 7])};
  return __builtin_bit_cast(bf16x8, p);
}
DI bf16x8 trread2(unsigned a0, unsigned a1) {
  s16x4 lo = __builtin_amdgcn_ds_read_tr16_b64_v4i16((LAS s16x4*)(uintptr_t)a0);
  s16x4 hi = __builtin_amdgcn_ds_read_tr16_b64_v4i16((LAS s16x4*)(uintptr_t)a1);
  return __builtin_bit_cast(bf16x8, __builtin_shufflevector(lo, hi, 0, 1, 2, 3, 4, 5, 6, 7));
}
DI bf16x8 ldsr16(unsigned a) { return *(const LAS bf16x8*)(uintptr_t)a; }
DI void ldsw16(unsigned a, bf16x8 v) { *(LAS bf16x8*)(uintptr_t)a = v; }
DI void glds16(const void* sbase, unsigned voff, unsigned lds_dst) {
  unsigned keep;
  asm volatile("s_mov_b32 %0, m0\n\ts_mov_b32 m0, %3\n\ts_nop 0\n\tglobal_load_lds_dwordx4 %1, %2\n\ts_mov_b32 m0, %0"
               : "=&s"(keep) : "v"(voff), "s"(sbase), "s"(lds_dst) : "memory");
}
template <int N> DI void wait_vm() { asm volatile("s_waitcnt vmcnt(%0)" ::"n"(N) : "memory"); }
template <int N> DI void wait_lgkm() { asm volatile("s_waitcnt lgkmcnt(%0)" ::"n"(N) : "memory"); }
DI void rawbar() { asm volatile("" ::: "memory"); __builtin_amdgcn_s_barrier(); asm volatile("" ::: "memory"); }
DI float swapmax(float m) { auto rr = __builtin_amdgcn_permlane32_swap(__float_as_uint(m), __float_as_uint(m), false, false); return fmaxf(__uint_as_float(rr[0]), __uint_as_float(rr[1])); }
DI float swapadd(float m) { auto rr = __builtin_amdgcn_permlane32_swap(__float_as_uint(m), __float_as_uint(m), false, false); return __uint_as_float(rr[0]) + __uint_as_float(rr[1]); }
DI unsigned opaque(unsigned v) { asm volatile("" : "+v"(v)); return v; }
DI float pinf(float v) { asm volatile("" : "+v"(v)); return v; }
DI float max3(float a, float b, float c) { float r; asm("v_max3_f32 %0, %1, %2, %3" : "=v"(r) : "v"(a), "v"(b), "v"(c)); return r; }
DI int fresh_lane() { int l; asm volatile("v_mbcnt_lo_u32_b32 %0, -1, 0\n\tv_mbcnt_hi_u32_b32 %0, -1, %0" : "=v"(l)); return l; }

template <typename F, int... I> DI void static_for_impl(F&& f, std::integer_sequence<int, I...>) { (f(std::integral_constant<int, I>{}), ...); }
template <int N, typename F> DI void static_for(F&& f) { static_for_impl(f, std::make_integer_sequence<int, N>{}); }

constexpr int NSLOT = 10, GS = NSLOT * 1024, LDSB = 16 * GS, NUNITS = 36, NST = 24;
DI int swz(int R) { return ((R & 3) << 2) | ((R >> 2) & 3); }
DI unsigned rowbase(int R, int hf) { return (unsigned)((R >> 2) * GS + (R & 3) * 256 + 16 * (hf ^ swz(R))); }
DI unsigned trW(int lane, int dt, int t) {
  const int hf = lane >> 5, q4 = (lane & 15) >> 2, p4 = lane & 3, blk = (lane >> 4) & 1;
  const int pc = ((p4 & 1) << 1) | (p4 >> 1);
  return (unsigned)((2 * hf + t) * GS + q4 * 256 + 16 * (((dt ^ q4) << 2) | ((2 * blk + (pc >> 1)) ^ (2 * hf + t))) + 8 * (pc & 1));
}
DI unsigned trV(int lane, int dt, int t) {
  const int hf = lane >> 5, q4 = (lane & 15) >> 2, p4 = lane & 3, blk = (lane >> 4) & 1;
  return (unsigned)((2 * t + hf) * GS + q4 * 256 + 16 * (((dt ^ q4) << 2) | ((2 * blk + (p4 >> 1)) ^ (2 * t + hf))) + 8 * (p4 & 1));
}

#define WLA 4
#define LDEPTH 3
#define TK -1
#define TV -1
#define TW 27
constexpr int cu(int t) { return t < 8 ? (t / 2) * 3 + 1 + (t & 1) : t + 4; }
constexpr int xu(int t) { return (t / 2) * 3; }
constexpr int stage_of(int u) { for (int t = 0; t < NST; ++t) if (cu(t) == u) return t; return -1; }
#define VPOS 8
#define EPOS 34
#define RELMASK 0x1ffffff
#define QPOS 15
struct Plan { int order[NUNITS], slot[NUNITS], need[NUNITS], qslot, qrel, e0, e1, erel; bool rel[26]; };
constexpr int last_use(int u) { return u < 12 ? (u % 3 == 0 ? (u / 3) * 2 + 1 : stage_of(u)) : (u < 28 ? u - 4 : 99); }
constexpr Plan make_plan() {
  Plan P{};
  int n = 0;
  for (int u = 0; u < 12 + VPOS; ++u) P.order[n++] = u;
  P.order[n++] = 20; P.order[n++] = 21;
  for (int u = 12 + VPOS; u < 20; ++u) P.order[n++] = u;
  for (int u = 22; u < 36; ++u) P.order[n++] = u;
  int fr[9] = {-1, -1, -1, -1, -1, -1, -1, -1, -1};
  auto take = [&](int until) { int bs = 0; for (int k = 1; k < 9; ++k) if (fr[k] < fr[bs]) bs = k; int f = fr[bs]; fr[bs] = until; return bs * 100 + (f + 1); };
  int run = -1;
  for (int p = 0; p < NUNITS; ++p) {
    if (p == QPOS) { int t = take(8); P.qslot = t / 100; P.qrel = t % 100 - 1; }
    if (p == EPOS) { int t = take(24); P.e0 = t / 100; P.erel = t % 100 - 1; t = take(24); P.e1 = t / 100; if (t % 100 - 1 > P.erel) P.erel = t % 100 - 1; }
    const int u = P.order[p];
    int t = take(last_use(u));
    P.slot[u] = t / 100;
    int f = t % 100 - 1;
    while (f >= 0 && !((RELMASK >> f) & 1)) ++f;
    if (f > run) { run = f; P.need[p] = f; P.rel[f] = true; } else P.need[p] = -1;
  }
  if (P.qrel >= 0) P.rel[P.qrel] = true;
  P.rel[P.erel] = true;
  return P;
}
constexpr Plan PL = make_plan();
static_assert(PL.qrel <= 6 && PL.erel <= 22, "plan");
constexpr int ntouch(int p) { return (p == TK ? 1 : 0) + (p == TV ? 1 : 0) + (p == TW ? 1 : 0); }
constexpr int ntouch_in(int q, int last) { int n = 0; for (int p = q; p <= last; ++p) n += ntouch(p); return n; }
constexpr unsigned soff(int u) { return (unsigned)PL.slot[u] * 1024u; }
constexpr unsigned exc(int k) { return (unsigned)((k >> 1) * GS + ((k & 1) ? PL.e1 : PL.e0) * 1024); }
constexpr unsigned FRG = PL.qslot * 1024, MISC = 9216;
constexpr unsigned F_CNT = MISC, F_WO = MISC + 96, F_Q = MISC + 112, F_O1 = MISC + 128, F_O2 = MISC + 144, F_REL = MISC + 160;

constexpr unsigned WS_O = 0, WS_P = 4u << 20, WS_F1 = 12u << 20, WS_F2 = WS_F1 + 65536, WS_EP = WS_F2 + 65536;

DI unsigned ldsflag(unsigned a) { return *(volatile const LAS unsigned*)(uintptr_t)a; }
DI void cfence() { asm volatile("" ::: "memory"); }
DI void spin_flag(unsigned a, unsigned want) {
  for (unsigned s = 0; s < (1u << 22); ++s) { if ((unsigned)__builtin_amdgcn_readfirstlane(ldsflag(a)) == want) break; }
  cfence();
}
#define LSLEEP 1
DI void spin_flag_slow(unsigned a, unsigned want) {
  for (unsigned s = 0; s < (1u << 20); ++s) { if ((unsigned)__builtin_amdgcn_readfirstlane(ldsflag(a)) == want) break; if (LSLEEP) __builtin_amdgcn_s_sleep(LSLEEP); }
  cfence();
}
constexpr unsigned ALL4 = 0x01010101u;
DI void lds_set(unsigned a) { cfence(); *(volatile LAS unsigned char*)(uintptr_t)a = 1; cfence(); }

#define WLA2 6
constexpr int isl(int m) { return m < 64 ? m - WLA : (m < 64 + WLA ? 63 : (64 + (m - 64 - WLA) / 2 > m - WLA2 ? 64 + (m - 64 - WLA) / 2 : m - WLA2)); }
#define FPRE 5
constexpr int fpre(int t) { int n = isl(8 * t) - FPRE; if (t > 8 && n < 64) n = 64; return n < 0 ? 0 : n; }
constexpr int first_read(int n) { for (int m = 0; m < 192; ++m) if (isl(m) >= n) return m; return 192; }
constexpr int qs_ks(int n) { return n < 56 ? (n % 8) / 2 : (n % 4); }
constexpr int qs_i(int n) { return n < 56 ? n % 2 : (n % 8) / 4; }
constexpr int pv_ks(int n) { return n < 184 ? (n % 8) / 4 : (n & 3) >> 1; }
constexpr int pv_dt(int n) { return n < 184 ? n % 4 : (n < 188 ? 2 : 0) + (n & 1); }

enum { OP_CHK, OP_EX, OP_ADD, OP_CVT };
struct VOp { int type, tile, idx; };
struct VSched { VOp op[512]; int start[194]; };
#define BQK 25
#define BPV 20
constexpr VSched make_sched() {
  VSched s{};
  VOp ex[48] = {};
  int k = 0;
  for (int e = 0; e < 16; ++e) {
    ex[k++] = VOp{OP_EX, 0, e};
    if (e >= 2) ex[k++] = VOp{OP_ADD, 0, e - 1};
    if (e >= 2 && (e & 1) == 0) ex[k++] = VOp{OP_CVT, 0, (e - 2) / 2};
  }
  ex[k++] = VOp{OP_ADD, 0, 15};
  ex[k++] = VOp{OP_CVT, 0, 7};
  ex[k++] = VOp{OP_CHK, 0, 0};
  const int len = k;
  int je = 0, ke = 0, no = 0, carry = 0;
  for (int n = 0; n < 192; ++n) {
    s.start[n] = no;
    if (n < 64) continue;
    int bud = (n < 128 ? BQK : BPV) + carry;
    while (je < 8 && n >= 64 + 8 * je + 7 + 2) {
      VOp o = ex[ke]; o.tile = je;
      int cost = o.type == OP_EX ? 11 : (o.type == OP_CHK ? 14 : 5);
      int dl = 128 + 8 * je - 2 - (len - 1 - ke) / 4;
      if (bud < cost && n < dl) break;
      s.op[no++] = o; bud -= cost;
      if (++ke == len) { ++je; ke = 0; }
    }
    carry = bud > 6 ? 6 : (bud < -6 ? -6 : bud);
  }
  s.start[192] = no; s.start[193] = je == 8 ? 1 : 0;
  return s;
}
constexpr VSched VS = make_sched();
static_assert(VS.start[193] == 1, "schedule incomplete");

#define STAMP(i)

__global__ __launch_bounds__(512, 2) void gqa_kernel(const u16* __restrict__ x, const u16* __restrict__ kc, const u16* __restrict__ vc,
                                                     const u16* __restrict__ wq, const u16* __restrict__ wo, char* __restrict__ ws, u16* __restrict__ out) {
  extern __shared__ __attribute__((aligned(16))) char lds[];
  const int wid8 = __builtin_amdgcn_readfirstlane(threadIdx.x >> 6);
  const int wid = wid8 & 3;
  const int bid = blockIdx.x;
  const int g = bid & 7, b = bid >> 5, hh = (bid >> 3) & 3, h = g * 4 + hh;
  const unsigned ldsb = (unsigned)(uintptr_t)(LAS char*)lds;
  gu32* const epw = (gu32*)(ws + WS_EP);

  if (wid8 >= 4) {
    const int lane = fresh_lane();
    unsigned voffx[4], voffw[4], voffk[4];
#pragma unroll
    for (int i = 0; i < 4; ++i) {
      unsigned row = wid * 16 + (lane >> 4) + 4 * i;
      unsigned ch = (lane & 15) ^ (((lane >> 4) << 2) | i);
      voffx[i] = row * 1024 + ch * 16; voffw[i] = row * 8192 + ch * 16; voffk[i] = row * 256 + ch * 16;
    }
    auto issue = [&](auto U) {
      constexpr int u = U;
      const unsigned dst = ldsb + soff(u) + wid * (4 * GS);
      if constexpr (u < 12) {
        const int q = ((u / 3) + (b >> 1) + hh) & 3; constexpr int rr = u % 3;
        if constexpr (rr == 0) { const u16* src = x + (size_t)b * 64 * 512 + q * 128;
#pragma unroll
          for (int i = 0; i < 4; ++i) glds16(src, voffx[i], dst + i * GS); }
        else { int wu = q * 2 + ((rr - 1) ^ (b & 1)); const u16* src = wq + (size_t)wu * 64 * 4096 + h * 128;
#pragma unroll
          for (int i = 0; i < 4; ++i) glds16(src, voffw[i], dst + i * GS); }
      } else if constexpr (u < 28) {
        int k = (u - 12 + 2 * hh) & 7; const u16* src = (u < 20 ? kc : vc) + ((size_t)(b * 8 + g) * 512 + k * 64) * 128;
#pragma unroll
        for (int i = 0; i < 4; ++i) glds16(src, voffk[i], dst + i * GS);
      } else {
        int k = (u - 28 + 2 * hh) & 7; const u16* src = wo + ((size_t)g * 512 + k * 64) * 512 + hh * 128;
#pragma unroll
        for (int i = 0; i < 4; ++i) glds16(src, voffx[i], dst + i * GS);
      }
    };
#define LST(i)
    const unsigned fb = opaque(ldsb), fbw = opaque(ldsb + wid);
    auto signal = [&](auto Q) {
      constexpr int u = PL.order[(int)Q];
      if constexpr (u < 28) { if constexpr (stage_of(u) >= 0) { lds_set(fbw + F_CNT + 4 * stage_of(u)); LST(stage_of(u)); } }
      else if constexpr (u & 1) { lds_set(fbw + F_WO + 4 * ((u - 28) >> 1)); LST(25 + ((u - 28) >> 1)); }
    };
    unsigned tch[3] = {0, 0, 0};
    auto post = [&](auto P) {
      constexpr int p = P;
      issue(std::integral_constant<int, PL.order[p]>{});
      if constexpr (p == TK) {
        const char* tp = (const char*)(kc + ((size_t)(b * 8 + g) * 512 + hh * 128) * 128) + (wid * 64 + lane) * 128;
        asm volatile("global_load_dword %0, %1, off" : "=v"(tch[0]) : "v"(tp) : "memory");
      }
      if constexpr (p == TV) {
        const char* tp = (const char*)(vc + ((size_t)(b * 8 + g) * 512 + hh * 128) * 128) + (wid * 64 + lane) * 128;
        asm volatile("global_load_dword %0, %1, off" : "=v"(tch[1]) : "v"(tp) : "memory");
      }
      if constexpr (p == TW) {
        const char* tp = (const char*)(wo + (size_t)g * 512 * 512) + (bid >> 3) * 16384 + (wid * 64 + lane) * 64;
        asm volatile("global_load_dword %0, %1, off" : "=v"(tch[2]) : "v"(tp) : "memory");
      }
    };
    LST(31);
    static_for<LDEPTH - 1>([&](auto P) { post(P); });
    rawbar();
    int sdone = -1;
    unsigned relv = 0;
    static_for<NUNITS>([&](auto Q) {
      constexpr int q = Q, p = q + LDEPTH - 1;
      if constexpr (p < NUNITS) {
        constexpr int k = PL.need[p];
        if constexpr (k >= 0) {
          if ((unsigned)__builtin_amdgcn_readfirstlane(relv) != ALL4) {
            static_for<LDEPTH - 1>([&](auto I) {
              constexpr int qq = q + (int)I;
              wait_vm<(p - 1 - qq) * 4 + ntouch_in(qq, p - 1)>();
              if (qq > sdone) signal(std::integral_constant<int, qq>{});
            });
            sdone = p - 1;
            spin_flag_slow(fb + F_REL + 4 * k, ALL4);
          }
          cfence();
        }
        if constexpr (p + 1 < NUNITS) { constexpr int k1 = PL.need[p + 1]; if constexpr (k1 >= 0) relv = ldsflag(fb + F_REL + 4 * k1); }
        post(std::integral_constant<int, p>{});
      }
      constexpr int last = p < NUNITS ? p : NUNITS - 1;
      wait_vm<(last - q) * 4 + ntouch_in(q, last)>();
      if (q > sdone) signal(Q);
    });
    wait_vm<0>();
    asm volatile("" :: "v"(tch[0]), "v"(tch[1]), "v"(tch[2]));
    if (bid == 0 && wid == 0) {
      const unsigned tag = (unsigned)__builtin_amdgcn_readfirstlane(__hip_atomic_load(epw, RLX_AGENT)) + 1u;
      gu32* f1 = (gu32*)(ws + WS_F1) + lane * 32;
      for (unsigned spins = 0; spins < (1u << 17); ++spins) {
        bool ok = true;
#pragma unroll
        for (int i = 0; i < 16; ++i) ok &= __hip_atomic_load(f1 + i, RLX_AGENT) == tag;
        if (__all(ok)) break;
        __builtin_amdgcn_s_sleep(2);
      }
      if (lane == 0) __hip_atomic_store(epw, tag, RLX_AGENT);
    }
    return;
  }

  __builtin_amdgcn_s_setprio(2);
  const int r = wid & 1, c = wid >> 1;
  STAMP(0);
  unsigned tag = 0;
  unsigned xa[8], wa[2][2][2], ka[8], va[4][2], lin = 0, fb = 0, fbw = 0;
  {
    const int lane = fresh_lane();
    if (wid == 0) { u32x2 z = {0u, 0u}; *(LAS u32x2*)(uintptr_t)(ldsb + MISC + lane * 8) = z; }
    wait_lgkm<0>();
    rawbar();
    const unsigned xb = (ldsb + rowbase(32 * r + (lane & 31), lane >> 5)) ^ (unsigned)((b & 1) * 128);
#pragma unroll
    for (int j = 0; j < 8; ++j) xa[j] = opaque(xb ^ (unsigned)(32 * j));
#pragma unroll
    for (int i = 0; i < 2; ++i)
#pragma unroll
      for (int t = 0; t < 2; ++t) { wa[i][t][0] = opaque(ldsb + trW(lane, 2 * c + i, t)); wa[i][t][1] = opaque(ldsb + trW(lane, 2 * c + i, t) + 8u * GS); }
    const unsigned kb = ldsb + rowbase(32 * c + (lane & 31), lane >> 5);
#pragma unroll
    for (int j = 0; j < 8; ++j) ka[j] = opaque(kb ^ (unsigned)(32 * (j ^ (4 * c))));
#pragma unroll
    for (int dt = 0; dt < 4; ++dt)
#pragma unroll
      for (int tt = 0; tt < 2; ++tt) va[dt][tt] = opaque(ldsb + trV(lane, dt ^ (2 * c), tt) + (unsigned)c * (8u * GS));
    lin = opaque(ldsb + lane * 16);
    fb = opaque(ldsb); fbw = opaque(ldsb + wid);
  }
  const unsigned epv = __hip_atomic_load(epw, RLX_AGENT);
  const unsigned own4 = (unsigned)wid * (4 * GS), oth4 = (unsigned)(wid ^ 2) * (4 * GS);

  bf16x8 fr[192], fx[32], qf[8], pf[8][2];
  f32x16 qacc[2] = {}, sacc[8], oacc[4] = {};
  float pv[8][16];
  unsigned pw[8][8], cntv[NST];
  float ts[8], mref = 0.f, alph[8], lsum = 0.f;
  unsigned long long slow = 0;
  constexpr float CS = 0.088388347648318440f * 1.4426950408889634f;
  constexpr float THI = 1099511627776.0f, TLO = 1.0f / 1099511627776.0f;

  auto issue_reads = [&](auto N) {
    constexpr int n = N, t = n / 8;
    if constexpr (n < 64) {
      constexpr int ks = qs_ks(n), i = qs_i(n);
      constexpr unsigned so = soff(cu(t)) + (4 * (ks & 1)) * GS;
      if constexpr (i == 0) fx[t * 4 + ks] = ldsr16(xa[(t & 1) * 4 + ks] + soff(xu(t)));
      fr[n] = trread2(wa[i][0][ks >> 1] + so, wa[i][1][ks >> 1] + so);
    } else if constexpr (n < 128) {
      fr[n] = ldsr16(ka[n % 8] + soff(cu(t)));
    } else {
      constexpr unsigned so = soff(cu(t)) + (4 * pv_ks(n)) * GS;
      fr[n] = trread2(va[pv_dt(n)][0] + so, va[pv_dt(n)][1] + so);
    }
  };
  auto wait_stage = [&](auto T) {
    constexpr int t = T;
    if (__builtin_expect((unsigned)__builtin_amdgcn_readfirstlane(cntv[t]) != ALL4, 0)) spin_flag(fb + F_CNT + 4 * t, ALL4);
    cfence();
  };
  auto qpack = [&](int i) {
    f32x16 t = qacc[i] * CS;
#pragma unroll
    for (int s = 0; s < 2; ++s) { qf[2 * i + s] = pack8(t, s); ldsw16(lin + own4 + (unsigned)(2 * i + s) * GS + FRG, qf[2 * i + s]); }
  };
  auto vop = [&](auto K) {
    constexpr VOp o = VS.op[(int)K];
    constexpr int j = o.tile, e = o.idx;
    if constexpr (o.type == OP_CHK) {
      alph[j] = 1.0f;
      const bool bad = j == 0 ? !(ts[0] >= TLO && ts[0] <= THI) : !(ts[j] <= THI);
      if (__builtin_expect((slow | __builtin_amdgcn_ballot_w64(bad)) != 0, 0)) {
        float m = sacc[j][0];
#pragma unroll
        for (int i = 1; i < 16; ++i) m = fmaxf(m, sacc[j][i]);
        m = swapmax(m);
        const float mn = j == 0 ? m : fmaxf(mref, m);
        if constexpr (j > 0) { alph[j] = __builtin_amdgcn_exp2f(mref - mn); lsum *= alph[j]; }
        mref = mn; slow = 1;
        f32x16 p; float sum = 0.f;
#pragma unroll
        for (int i = 0; i < 16; ++i) { p[i] = __builtin_amdgcn_exp2f(sacc[j][i] - mn); sum += p[i]; }
        ts[j] = sum; pf[j][0] = pack8(p, 0); pf[j][1] = pack8(p, 1);
      }
      lsum = pinf(lsum + ts[j]);
    } else if constexpr (o.type == OP_EX) {
      pv[j][e] = pinf(__builtin_amdgcn_exp2f(sacc[j][e]));
    } else if constexpr (o.type == OP_ADD) {
      if constexpr (e == 1) ts[j] = pinf(pv[j][0] + pv[j][1]); else ts[j] = pinf(ts[j] + pv[j][e]);
    } else {
      pw[j][e] = opaque(pk2(pv[j][2 * e], pv[j][2 * e + 1]));
      if constexpr ((e & 3) == 3) { u32x4 p = {pw[j][e - 3], pw[j][e - 2], pw[j][e - 1], pw[j][e]}; pf[j][e >> 2] = __builtin_bit_cast(bf16x8, p); }
    }
  };
  auto osend = [&](int i) {
#pragma unroll
    for (int gq = 0; gq < 4; ++gq) {
      f32x4 v = {oacc[2 + i][4 * gq], oacc[2 + i][4 * gq + 1], oacc[2 + i][4 * gq + 2], oacc[2 + i][4 * gq + 3]};
      *(LAS f32x4*)(uintptr_t)(lin + own4 + exc(i * 4 + gq)) = v;
    }
  };
  float lw = 0.f;
  unsigned relq = 0;
#define CST(i)
  unsigned cst[32];

  spin_flag(fb + F_CNT, ALL4);
  STAMP(1);
  static_for<WLA>([&](auto N) { issue_reads(N); });
  SB();
  static_for<192>([&](auto N) {
    constexpr int n = N, t = n / 8;
    if constexpr (n >= 136 && n % 8 == 0) {
      if (__builtin_expect(slow != 0, 0)) {
#pragma unroll
        for (int dt = 0; dt < 4; ++dt) oacc[dt] *= alph[t - 16];
      }
    }
    if constexpr (n % 8 == 0) { if constexpr (n % 32 == 0) { CST(t); } else cst[t] = cst[t - 1]; }
    if constexpr (n < 64) {
      constexpr int ks = qs_ks(n), i = qs_i(n);
      qacc[i] = MFMA32(fr[n], fx[t * 4 + ks], qacc[i]);
    } else if constexpr (n < 128) {
      constexpr int j = t - 8, k = n % 8;
      if constexpr (k == 0) { f32x16 z = {}; sacc[j] = MFMA32(fr[n], qf[0], z); }
      else sacc[j] = MFMA32(fr[n], qf[k], sacc[j]);
    } else {
      oacc[pv_dt(n)] = MFMA32(fr[n], pf[t - 16][pv_ks(n)], oacc[pv_dt(n)]);
    }
    SB();
    if constexpr (n == 48) tag = (unsigned)__builtin_amdgcn_readfirstlane(epv) + 1u;
    static_for<3>([&](auto I) {
      constexpr int m = first_read(n) + (int)I;
      if constexpr (m < 192 && n != 63) {
        if constexpr (isl(m) == n) {
          if constexpr (m % 8 == 0) wait_stage(std::integral_constant<int, m / 8>{});
          issue_reads(std::integral_constant<int, m>{});
          if constexpr (m % 8 == 7) { if constexpr (PL.rel[m / 8]) lds_set(fbw + F_REL + 4 * (m / 8)); }
        }
      }
    });
    static_for<NST>([&](auto T) { constexpr int tt = T; if constexpr (tt > 0 && tt != 8 && fpre(tt) == n) cntv[tt] = ldsflag(fb + F_CNT + 4 * tt); });
    if constexpr (n == 55 && PL.qrel >= 0) relq = ldsflag(fb + F_REL + 4 * PL.qrel);
    if constexpr (n == 61) {
      if constexpr (PL.qrel >= 0) { if (__builtin_expect((unsigned)__builtin_amdgcn_readfirstlane(relq) != ALL4, 0)) spin_flag(fb + F_REL + 4 * PL.qrel, ALL4); }
      cfence();
      qpack(0);
    }
    if constexpr (n == 184) relq = ldsflag(fb + F_REL + 4 * PL.erel);
    if constexpr (n == 63) {
      qpack(1);
      lds_set(fb + F_Q + 4 * wid);
      CST(26);
      spin_flag(fb + F_Q + 4 * (wid ^ 2), 1u);
      CST(27);
#pragma unroll
      for (int k = 0; k < 4; ++k) qf[4 + k] = ldsr16(lin + oth4 + (unsigned)k * GS + FRG);
      spin_flag(fb + F_CNT + 4 * 8, ALL4);
      static_for<WLA>([&](auto I) { issue_reads(std::integral_constant<int, 64 + (int)I>{}); });
    }
    if constexpr (n >= 64) static_for<VS.start[n + 1] - VS.start[n]>([&](auto K) { vop(std::integral_constant<int, VS.start[n] + (int)K>{}); });
    if constexpr (n == 188) {
      lw = swapadd(lsum);
      f32x2 st = {mref, lw};
      *(LAS f32x2*)(uintptr_t)(lin + (unsigned)(1 + wid) * GS + MISC) = st;
    }
    if constexpr (n == 189) {
      if (__builtin_expect((unsigned)__builtin_amdgcn_readfirstlane(relq) != ALL4, 0)) spin_flag(fb + F_REL + 4 * PL.erel, ALL4);
      cfence();
      osend(0);
    }
    if constexpr (n == 190) { osend(1); lds_set(fb + F_O1 + 4 * wid); }
    SB();
  });
  STAMP(30);
  CST(24);
  spin_flag(fb + F_O1 + 4 * (wid ^ 2), 1u);
  STAMP(31);
  const int lane = fresh_lane();
  const __amdgpu_buffer_rsrc_t wsr = __builtin_amdgcn_make_buffer_rsrc(ws, 0, 16u << 20, 0x00020000);
  bf16x8 of[4][8];
  {
    const f32x2 so2 = *(const LAS f32x2*)(uintptr_t)(lin + (unsigned)(1 + (wid ^ 2)) * GS + MISC);
    f32x4 v[8];
#pragma unroll
    for (int k = 0; k < 8; ++k) v[k] = *(const LAS f32x4*)(uintptr_t)(lin + oth4 + exc(k));
    const float M = fmaxf(mref, so2[0]);
    const float bs = __builtin_amdgcn_exp2f(mref - M), bo = __builtin_amdgcn_exp2f(so2[0] - M);
    const float inv = 1.0f / (lw * bs + so2[1] * bo);
    const float fs = bs * inv, fo2 = bo * inv;
    const unsigned goff = WS_O + (unsigned)(((bid * 2 + r) * 4 + 2 * c) * 2) * 1024 + lane * 16;
#pragma unroll
    for (int i = 0; i < 2; ++i) {
      f32x16 t;
#pragma unroll
      for (int gq = 0; gq < 4; ++gq)
#pragma unroll
        for (int e = 0; e < 4; ++e) t[4 * gq + e] = fmaf(oacc[i][4 * gq + e], fs, v[i * 4 + gq][e] * fo2);
#pragma unroll
      for (int s = 0; s < 2; ++s) {
        of[0][i * 2 + s] = pack8(t, s);
        __builtin_amdgcn_raw_buffer_store_b128(__builtin_bit_cast(u32x4, of[0][i * 2 + s]), wsr, goff + (i * 2 + s) * 1024, 0, 16);
      }
    }
    cfence();
#pragma unroll
    for (int k = 0; k < 4; ++k) ldsw16(lin + oth4 + exc(k), of[0][k]);
    lds_set(fb + F_O2 + 4 * wid);
  }
  unsigned ob[2][2], oa[2][2][2][2];
#pragma unroll
  for (int ci = 0; ci < 2; ++ci)
#pragma unroll
    for (int tt = 0; tt < 2; ++tt) ob[ci][tt] = ldsb + trV(lane, 2 * c + ci, tt);
  auto oaddr = [&](auto JJ, auto HS) {
    constexpr int jj = JJ, hs = HS;
    const unsigned so = c ? soff(28 + 2 * jj + (hs ^ 1)) : soff(28 + 2 * jj + hs);
#pragma unroll
    for (int ci = 0; ci < 2; ++ci)
#pragma unroll
      for (int tt = 0; tt < 2; ++tt) { oa[hs][ci][tt][0] = opaque(ob[ci][tt] + so); oa[hs][ci][tt][1] = opaque(ob[ci][tt] + so + 8u * GS); }
  };
  STAMP(32);
  asm volatile("s_waitcnt vmcnt(0)" ::: "memory");
  if (lane == 0) __hip_atomic_store((gu32*)(ws + WS_F1) + (b * 8 + g) * 32 + hh * 4 + wid, tag, RLX_AGENT);
  STAMP(33);
  spin_flag(fb + F_O2 + 4 * (wid ^ 2), 1u);
#pragma unroll
  for (int k = 0; k < 4; ++k) of[0][4 + k] = ldsr16(lin + own4 + exc(k));
  lds_set(fbw + F_REL + 4 * 24);
  spin_flag(fb + F_WO, ALL4);
  bf16x8 fo[64];
  f32x16 pacc[2] = {};
  auto oreads = [&](auto M) {
    constexpr int m = M, ks4 = (m % 8) / 2, ci = m % 2, hs = (m / 8) % 2;
    constexpr unsigned so = (4 * (ks4 & 1)) * GS;
    if constexpr (m % 16 == 0 && m > 0) spin_flag(fb + F_WO + 4 * (m / 16), ALL4);
    if constexpr (m % 8 == 0) oaddr(std::integral_constant<int, m / 16>{}, std::integral_constant<int, hs>{});
    fo[m] = trread2(oa[hs][ci][0][ks4 >> 1] + so, oa[hs][ci][1][ks4 >> 1] + so);
  };
  static_for<WLA>([&](auto M) { oreads(M); });
  STAMP(34);
  {
    gu32* f1 = (gu32*)(ws + WS_F1) + (b * 8 + g) * 32 + ((lane >> 1) & 3) * 4 + (lane & 1) * 2 + r;
    for (unsigned spins = 0; spins < (1u << 17); ++spins) {
      if (__all(__hip_atomic_load(f1, RLX_AGENT) == tag)) break;
    }
    asm volatile("" ::: "memory");
    STAMP(35);
#pragma unroll
    for (int j2 = 1; j2 < 4; ++j2) {
      const int sb = (bid & ~24) | (((hh + j2) & 3) << 3);
#pragma unroll
      for (int k = 0; k < 8; ++k)
        of[j2][k] = __builtin_bit_cast(bf16x8, __builtin_amdgcn_raw_buffer_load_b128(wsr, WS_O + (unsigned)((sb * 2 + r) * 8 + (k ^ (4 * c))) * 1024 + lane * 16, 0, 16));
    }
  }
  SB();
  static_for<64>([&](auto M) {
    constexpr int m = M, jj = m / 16, half = (m / 8) % 2, ks4 = (m % 8) / 2, ci = m % 2;
    pacc[ci] = MFMA32(fo[m], of[jj][half * 4 + ks4], pacc[ci]);
    SB();
    if constexpr (m + WLA < 64) oreads(std::integral_constant<int, m + WLA>{});
    SB();
  });
  CST(25);
  STAMP(36);
#pragma unroll
  for (int ci = 0; ci < 2; ++ci) {
    const int cons = (bid & ~7) | (2 * (2 * c + ci) + r);
#pragma unroll
    for (int gq = 0; gq < 4; ++gq) {
      f32x4 v = {pacc[ci][4 * gq], pacc[ci][4 * gq + 1], pacc[ci][4 * gq + 2], pacc[ci][4 * gq + 3]};
      __builtin_amdgcn_raw_buffer_store_b128(__builtin_bit_cast(u32x4, v), wsr, WS_P + (unsigned)((cons * 8 + g) * 4 + gq) * 1024 + lane * 16, 0, 16);
    }
  }
  asm volatile("s_waitcnt vmcnt(0)" ::: "memory");
  if (lane < 2) {
    const int cons = (bid & ~7) | (2 * (2 * c + lane) + r);
    __hip_atomic_store((gu32*)(ws + WS_F2) + cons * 32 + g, tag, RLX_AGENT);
  }
  STAMP(37);
  {
    gu32* f2 = (gu32*)(ws + WS_F2) + bid * 32 + (lane & 7);
    for (unsigned spins = 0; spins < (1u << 17); ++spins) {
      if (__all(__hip_atomic_load(f2, RLX_AGENT) == tag)) break;
    }
    asm volatile("" ::: "memory");
    STAMP(38);
    f32x4 pp[8];
#pragma unroll
    for (int p = 0; p < 8; ++p)
      pp[p] = __builtin_bit_cast(f32x4, __builtin_amdgcn_raw_buffer_load_b128(wsr, WS_P + (unsigned)((bid * 8 + p) * 4 + wid) * 1024 + lane * 16, 0, 16));
    f32x4 s = ((pp[0] + pp[1]) + (pp[2] + pp[3])) + ((pp[4] + pp[5]) + (pp[6] + pp[7]));
    const int ct = g >> 1, rr = g & 1;
    u32x2 o2 = {pk2(s[0], s[1]), pk2(s[2], s[3])};
    *(u32x2*)(out + (size_t)(b * 64 + rr * 32 + (lane & 31)) * 512 + hh * 128 + ct * 32 + 8 * wid + 4 * (lane >> 5)) = o2;
  }
  STAMP(39);
}

extern "C" void kernel_launch(void* const* d_in, const int* in_sizes, int n_in, void* d_out, int out_size, void* d_ws, size_t ws_size, hipStream_t stream) {
  (void)hipFuncSetAttribute((const void*)gqa_kernel, hipFuncAttributeMaxDynamicSharedMemorySize, LDSB);
  gqa_kernel<<<256, 512, LDSB, stream>>>((const u16*)d_in[0], (const u16*)d_in[1], (const u16*)d_in[2], (const u16*)d_in[3], (const u16*)d_in[4], (char*)d_ws, (u16*)d_out);
}
```

```cpp
#include <hip/hip_runtime.h>
#include <hip/hip_bf16.h>
#include <utility>

typedef unsigned short u16;
typedef __bf16 bf16x8 __attribute__((ext_vector_type(8)));
typedef __bf16 bf16x2 __attribute__((ext_vector_type(2)));
typedef short s16x4 __attribute__((ext_vector_type(4)));
typedef float f32x16 __attribute__((ext_vector_type(16)));
typedef float f32x4 __attribute__((ext_vector_type(4)));
typedef float f32x2 __attribute__((ext_vector_type(2)));
typedef unsigned u32x4 __attribute__((ext_vector_type(4)));
typedef unsigned u32x2 __attribute__((ext_vector_type(2)));
#define LAS __attribute__((address_space(3)))
#define GAS __attribute__((address_space(1)))
typedef GAS unsigned gu32;
#define DI __device__ __forceinline__
#define MFMA32(a, b, c) __builtin_amdgcn_mfma_f32_32x32x16_bf16((a), (b), (c), 0, 0, 0)
#define RLX_AGENT __ATOMIC_RELAXED, __HIP_MEMORY_SCOPE_AGENT
#define SB() __builtin_amdgcn_sched_barrier(0)

DI unsigned pk2(float a, float b) { f32x2 v = {a, b}; bf16x2 r = __builtin_convertvector(v, bf16x2); return __builtin_bit_cast(unsigned, r); }
DI bf16x8 pack8(const f32x16& x, int s) {
  u32x4 p = {pk2(x[8 * s], x[8 * s + 1]), pk2(x[8 * s + 2], x[8 * s + 3]), pk2(x[8 * s + 4], x[8 * s + 5]), pk2(x[8 * s + 6], x[8 * s + 7])};
  return __builtin_bit_cast(bf16x8, p);
}
DI bf16x8 trread2(unsigned a0, unsigned a1) {
  s16x4 lo = __builtin_amdgcn_ds_read_tr16_b64_v4i16((LAS s16x4*)(uintptr_t)a0);
  s16x4 hi = __builtin_amdgcn_ds_read_tr16_b64_v4i16((LAS s16x4*)(uintptr_t)a1);
  return __builtin_bit_cast(bf16x8, __builtin_shufflevector(lo, hi, 0, 1, 2, 3, 4, 5, 6, 7));
}
DI bf16x8 ldsr16(unsigned a) { return *(const LAS bf16x8*)(uintptr_t)a; }
DI void ldsw16(unsigned a, bf16x8 v) { *(LAS bf16x8*)(uintptr_t)a = v; }
DI void glds16(const void* sbase, unsigned voff, unsigned lds_dst) {
  unsigned keep;
  asm volatile("s_mov_b32 %0, m0\n\ts_mov_b32 m0, %3\n\ts_nop 0\n\tglobal_load_lds_dwordx4 %1, %2\n\ts_mov_b32 m0, %0"
               : "=&s"(keep) : "v"(voff), "s"(sbase), "s"(lds_dst) : "memory");
}
template <int N> DI void wait_vm() { asm volatile("s_waitcnt vmcnt(%0)" ::"n"(N) : "memory"); }
template <int N> DI void wait_lgkm() { asm volatile("s_waitcnt lgkmcnt(%0)" ::"n"(N) : "memory"); }
DI void rawbar() { asm volatile("" ::: "memory"); __builtin_amdgcn_s_barrier(); asm volatile("" ::: "memory"); }
DI float swapmax(float m) { auto rr = __builtin_amdgcn_permlane32_swap(__float_as_uint(m), __float_as_uint(m), false, false); return fmaxf(__uint_as_float(rr[0]), __uint_as_float(rr[1])); }
DI float swapadd(float m) { auto rr = __builtin_amdgcn_permlane32_swap(__float_as_uint(m), __float_as_uint(m), false, false); return __uint_as_float(rr[0]) + __uint_as_float(rr[1]); }
DI unsigned opaque(unsigned v) { asm volatile("" : "+v"(v)); return v; }
DI float pinf(float v) { asm volatile("" : "+v"(v)); return v; }
DI float max3(float a, float b, float c) { float r; asm("v_max3_f32 %0, %1, %2, %3" : "=v"(r) : "v"(a), "v"(b), "v"(c)); return r; }
DI int fresh_lane() { int l; asm volatile("v_mbcnt_lo_u32_b32 %0, -1, 0\n\tv_mbcnt_hi_u32_b32 %0, -1, %0" : "=v"(l)); return l; }

template <typename F, int... I> DI void static_for_impl(F&& f, std::integer_sequence<int, I...>) { (f(std::integral_constant<int, I>{}), ...); }
template <int N, typename F> DI void static_for(F&& f) { static_for_impl(f, std::make_integer_sequence<int, N>{}); }

constexpr int NSLOT = 10, GS = NSLOT * 1024, LDSB = 16 * GS, NUNITS = 36, NST = 24;
DI int swz(int R) { return ((R & 3) << 2) | ((R >> 2) & 3); }
DI unsigned rowbase(int R, int hf) { return (unsigned)((R >> 2) * GS + (R & 3) * 256 + 16 * (hf ^ swz(R))); }
DI unsigned trW(int lane, int dt, int t) {
  const int hf = lane >> 5, q4 = (lane & 15) >> 2, p4 = lane & 3, blk = (lane >> 4) & 1;
  const int pc = ((p4 & 1) << 1) | (p4 >> 1);
  return (unsigned)((2 * hf + t) * GS + q4 * 256 + 16 * (((dt ^ q4) << 2) | ((2 * blk + (pc >> 1)) ^ (2 * hf + t))) + 8 * (pc & 1));
}
DI unsigned trV(int lane, int dt, int t) {
  const int hf = lane >> 5, q4 = (lane & 15) >> 2, p4 = lane & 3, blk = (lane >> 4) & 1;
  return (unsigned)((2 * t + hf) * GS + q4 * 256 + 16 * (((dt ^ q4) << 2) | ((2 * blk + (p4 >> 1)) ^ (2 * t + hf))) + 8 * (p4 & 1));
}

#define WLA 4
#define LDEPTH 3
#define TK -1
#define TV -1
#define TW 27
constexpr int cu(int t) { return t < 8 ? (t / 2) * 3 + 1 + (t & 1) : t + 4; }
constexpr int xu(int t) { return (t / 2) * 3; }
constexpr int stage_of(int u) { for (int t = 0; t < NST; ++t) if (cu(t) == u) return t; return -1; }
#define VPOS 8
#define EPOS 34
#define RELMASK 0x1ffffff
#define QPOS 15
struct Plan { int order[NUNITS], slot[NUNITS], need[NUNITS], qslot, qrel, e0, e1, erel; bool rel[26]; };
constexpr int last_use(int u) { return u < 12 ? (u % 3 == 0 ? (u / 3) * 2 + 1 : stage_of(u)) : (u < 28 ? u - 4 : 99); }
constexpr Plan make_plan() {
  Plan P{};
  int n = 0;
  for (int u = 0; u < 12 + VPOS; ++u) P.order[n++] = u;
  P.order[n++] = 20; P.order[n++] = 21;
  for (int u = 12 + VPOS; u < 20; ++u) P.order[n++] = u;
  for (int u = 22; u < 36; ++u) P.order[n++] = u;
  int fr[9] = {-1, -1, -1, -1, -1, -1, -1, -1, -1};
  auto take = [&](int until) { int bs = 0; for (int k = 1; k < 9; ++k) if (fr[k] < fr[bs]) bs = k; int f = fr[bs]; fr[bs] = until; return bs * 100 + (f + 1); };
  int run = -1;
  for (int p = 0; p < NUNITS; ++p) {
    if (p == QPOS) { int t = take(8); P.qslot = t / 100; P.qrel = t % 100 - 1; }
    if (p == EPOS) { int t = take(99); P.e0 = t / 100; P.erel = t % 100 - 1; }
    const int u = P.order[p];
    int t = take(last_use(u));
    P.slot[u] = t / 100;
    int f = t % 100 - 1;
    while (f >= 0 && !((RELMASK >> f) & 1)) ++f;
    if (f > run) { run = f; P.need[p] = f; P.rel[f] = true; } else P.need[p] = -1;
  }
  if (P.qrel >= 0) P.rel[P.qrel] = true;
  P.rel[P.erel] = true;
  return P;
}
constexpr Plan PL = make_plan();
static_assert(PL.order[0] == 0 && PL.need[0] < 0 && PL.need[1] < 0 && PL.need[2] < 0 && PL.need[3] < 0 && PL.need[4] < 0 && PL.need[5] < 0 && LDEPTH <= 4, "no flag access before the barrier");
static_assert(PL.qrel <= 6 && PL.erel <= 22, "plan");
constexpr int ntouch(int p) { return (p == TK ? 1 : 0) + (p == TV ? 1 : 0) + (p == TW ? 1 : 0); }
constexpr int ntouch_in(int q, int last) { int n = 0; for (int p = q; p <= last; ++p) n += ntouch(p); return n; }
constexpr unsigned soff(int u) { return (unsigned)PL.slot[u] * 1024u; }
constexpr unsigned exc(int k) { return (unsigned)(k * GS + PL.e0 * 1024); }
constexpr unsigned FRG = PL.qslot * 1024, MISC = 9216;
constexpr unsigned F_CNT = MISC, F_WO = MISC + 96, F_Q = MISC + 112, F_O1 = MISC + 128, F_O2 = MISC + 144, F_REL = MISC + 160, F_P = MISC + 288;

constexpr unsigned WS_O = 0, WS_P = 4u << 20, WS_F1 = 12u << 20, WS_F2 = WS_F1 + 65536, WS_EP = WS_F2 + 65536;

DI unsigned next_tag(unsigned e) { return e * 0x9E3779B1u + 0x7F4A7C15u; }
#define CTL 20
#define LOADER_FIRST(x) __builtin_expect((x), 1)
DI unsigned ldsflag(unsigned a) { return *(volatile const LAS unsigned*)(uintptr_t)a; }
DI void cfence() { asm volatile("" ::: "memory"); }
DI void spin_flag(unsigned a, unsigned want) {
  for (unsigned s = 0; s < (1u << 22); ++s) { if ((unsigned)__builtin_amdgcn_readfirstlane(ldsflag(a)) == want) break; }
  cfence();
}
#define LSLEEP 1
DI void spin_flag_slow(unsigned a, unsigned want) {
  for (unsigned s = 0; s < (1u << 20); ++s) { if ((unsigned)__builtin_amdgcn_readfirstlane(ldsflag(a)) == want) break; if (LSLEEP) __builtin_amdgcn_s_sleep(LSLEEP); }
  cfence();
}
constexpr unsigned ALL4 = 0x01010101u;
DI void lds_set(unsigned a) { cfence(); *(volatile LAS unsigned char*)(uintptr_t)a = 1; cfence(); }

#define WLA2 6
constexpr int isl(int m) { return m < 64 ? m - WLA : (m < 64 + WLA ? 63 : (64 + (m - 64 - WLA) / 2 > m - WLA2 ? 64 + (m - 64 - WLA) / 2 : m - WLA2)); }
#define FPRE 5
constexpr int fpre(int t) { int n = isl(8 * t) - FPRE; if (t > 8 && n < 64) n = 64; return n < 0 ? 0 : n; }
constexpr int first_read(int n) { for (int m = 0; m < 192; ++m) if (isl(m) >= n) return m; return 192; }
constexpr int qs_ks(int n) { return n < 56 ? (n % 8) / 2 : (n % 4); }
constexpr int qs_i(int n) { return n < 56 ? n % 2 : (n % 8) / 4; }
constexpr int pv_ks(int n) { return n < 184 ? (n % 8) / 4 : (n & 3) >> 1; }
constexpr int pv_dt(int n) { return n < 184 ? n % 4 : (n < 188 ? 2 : 0) + (n & 1); }

constexpr int op_ks(int m) { return m < 48 ? (m % 16) / 4 : (m % 8) / 2; }
constexpr int op_ct(int m) { return m < 48 ? m % 4 : (m < 56 ? 2 : 0) + (m & 1); }
#define OLA0 8
#define OLA 12
constexpr int op_isl(int m) { return m < OLA0 ? -1 : ((m - OLA0) / 2 > m - OLA ? (m - OLA0) / 2 : m - OLA); }
constexpr int op_first(int n) { for (int m = OLA0; m < 64; ++m) if (op_isl(m) >= n) return m; return 64; }
enum { OP_CHK, OP_EX, OP_ADD, OP_CVT };
struct VOp { int type, tile, idx; };
struct VSched { VOp op[512]; int start[194]; };
#define BQK 25
#define BPV 20
constexpr VSched make_sched() {
  VSched s{};
  VOp ex[48] = {};
  int k = 0;
  for (int e = 0; e < 16; ++e) {
    ex[k++] = VOp{OP_EX, 0, e};
    if (e >= 2) ex[k++] = VOp{OP_ADD, 0, e - 1};
    if (e >= 2 && (e & 1) == 0) ex[k++] = VOp{OP_CVT, 0, (e - 2) / 2};
  }
  ex[k++] = VOp{OP_ADD, 0, 15};
  ex[k++] = VOp{OP_CVT, 0, 7};
  ex[k++] = VOp{OP_CHK, 0, 0};
  const int len = k;
  int je = 0, ke = 0, no = 0, carry = 0;
  for (int n = 0; n < 192; ++n) {
    s.start[n] = no;
    if (n < 64) continue;
    int bud = (n < 128 ? BQK : BPV) + carry;
    while (je < 8 && n >= 64 + 8 * je + 7 + 2) {
      VOp o = ex[ke]; o.tile = je;
      int cost = o.type == OP_EX ? 11 : (o.type == OP_CHK ? 14 : 5);
      int dl = 128 + 8 * je - 2 - (len - 1 - ke) / 4;
      if (bud < cost && n < dl) break;
      s.op[no++] = o; bud -= cost;
      if (++ke == len) { ++je; ke = 0; }
    }
    carry = bud > 6 ? 6 : (bud < -6 ? -6 : bud);
  }
  s.start[192] = no; s.start[193] = je == 8 ? 1 : 0;
  return s;
}
constexpr VSched VS = make_sched();
static_assert(VS.start[193] == 1, "schedule incomplete");

#define STAMP(i)

__global__ __launch_bounds__(512, 2) void gqa_kernel(const u16* __restrict__ x, const u16* __restrict__ kc, const u16* __restrict__ vc,
                                                     const u16* __restrict__ wq, const u16* __restrict__ wo, char* __restrict__ ws, u16* __restrict__ out) {
  extern __shared__ __attribute__((aligned(16))) char lds[];
  const int wid8 = __builtin_amdgcn_readfirstlane(threadIdx.x >> 6);
  const int wid = wid8 & 3;
  const int bid = blockIdx.x;
  const int g = bid & 7, b = bid >> 5, hh = (bid >> 3) & 3, h = g * 4 + hh;
  const unsigned ldsb = (unsigned)(uintptr_t)(LAS char*)lds;
  unsigned long long pc0;
  asm volatile("s_getpc_b64 %0" : "=s"(pc0));

  if (LOADER_FIRST(wid8 >= 4)) {
    const int lane = fresh_lane();
    unsigned voffx[4], voffw[4], voffk[4];
#pragma unroll
    for (int i = 0; i < 4; ++i) {
      unsigned row = wid * 16 + (lane >> 4) + 4 * i;
      unsigned ch = (lane & 15) ^ (((lane >> 4) << 2) | i);
      voffx[i] = row * 1024 + ch * 16; voffw[i] = row * 8192 + ch * 16; voffk[i] = row * 256 + ch * 16;
    }
    auto issue = [&](auto U) {
      constexpr int u = U;
      const unsigned dst = ldsb + soff(u) + wid * (4 * GS);
      if constexpr (u < 12) {
        const int q = ((u / 3) + (b >> 1) + hh) & 3; constexpr int rr = u % 3;
        if constexpr (rr == 0) { const u16* src = x + (size_t)b * 64 * 512 + q * 128;
#pragma unroll
          for (int i = 0; i < 4; ++i) glds16(src, voffx[i], dst + i * GS); }
        else { int wu = q * 2 + ((rr - 1) ^ (b & 1)); const u16* src = wq + (size_t)wu * 64 * 4096 + h * 128;
#pragma unroll
          for (int i = 0; i < 4; ++i) glds16(src, voffw[i], dst + i * GS); }
      } else if constexpr (u < 28) {
        int k = (u - 12 + 2 * hh) & 7; const u16* src = (u < 20 ? kc : vc) + ((size_t)(b * 8 + g) * 512 + k * 64) * 128;
#pragma unroll
        for (int i = 0; i < 4; ++i) glds16(src, voffk[i], dst + i * GS);
      } else {
        int k = (u - 28 + 2 * hh) & 7; const u16* src = wo + ((size_t)g * 512 + k * 64) * 512 + hh * 128;
#pragma unroll
        for (int i = 0; i < 4; ++i) glds16(src, voffx[i], dst + i * GS);
      }
    };
#define LST(i)
    const unsigned fb = opaque(ldsb), fbw = opaque(ldsb + wid);
    auto signal = [&](auto Q) {
      constexpr int u = PL.order[(int)Q];
      if constexpr (u < 28) { if constexpr (stage_of(u) >= 0) { lds_set(fbw + F_CNT + 4 * stage_of(u)); LST(stage_of(u)); } }
      else if constexpr (u & 1) { lds_set(fbw + F_WO + 4 * ((u - 28) >> 1)); LST(25 + ((u - 28) >> 1)); }
    };
    unsigned tch[3] = {0, 0, 0};
    auto post = [&](auto P) {
      constexpr int p = P;
      issue(std::integral_constant<int, PL.order[p]>{});
      if constexpr (p == TK) {
        const char* tp = (const char*)(kc + ((size_t)(b * 8 + g) * 512 + hh * 128) * 128) + (wid * 64 + lane) * 128;
        asm volatile("global_load_dword %0, %1, off" : "=v"(tch[0]) : "v"(tp) : "memory");
      }
      if constexpr (p == TV) {
        const char* tp = (const char*)(vc + ((size_t)(b * 8 + g) * 512 + hh * 128) * 128) + (wid * 64 + lane) * 128;
        asm volatile("global_load_dword %0, %1, off" : "=v"(tch[1]) : "v"(tp) : "memory");
      }
      if constexpr (p == TW) {
        const char* tp = (const char*)(wo + (size_t)g * 512 * 512) + (bid >> 3) * 16384 + (wid * 64 + lane) * 64;
        asm volatile("global_load_dword %0, %1, off" : "=v"(tch[2]) : "v"(tp) : "memory");
      }
    };
    LST(31);
    static_for<LDEPTH - 1>([&](auto P) { post(P); });
    int sdone = -1;
    unsigned relv = 0;
    static_for<NUNITS>([&](auto Q) {
      constexpr int q = Q, p = q + LDEPTH - 1;
      if constexpr (p < NUNITS) {
        constexpr int k = PL.need[p];
        if constexpr (k >= 0) {
          if ((unsigned)__builtin_amdgcn_readfirstlane(relv) != ALL4) {
            static_for<LDEPTH - 1>([&](auto I) {
              constexpr int qq = q + (int)I;
              wait_vm<(p - 1 - qq) * 4 + ntouch_in(qq, p - 1)>();
              if (qq > sdone) signal(std::integral_constant<int, qq>{});
            });
            sdone = p - 1;
            spin_flag_slow(fb + F_REL + 4 * k, ALL4);
          }
          cfence();
        }
        if constexpr (p + 1 < NUNITS) { constexpr int k1 = PL.need[p + 1]; if constexpr (k1 >= 0) relv = ldsflag(fb + F_REL + 4 * k1); }
        post(std::integral_constant<int, p>{});
      }
      constexpr int last = p < NUNITS ? p : NUNITS - 1;
      wait_vm<(last - q) * 4 + ntouch_in(q, last)>();
      if constexpr (q == 1) rawbar();
      if (q > sdone) signal(Q);
    });
    wait_vm<0>();
    asm volatile("" :: "v"(tch[0]), "v"(tch[1]), "v"(tch[2]));
    if (bid == 0 && wid == 0) {
      gu32* const epw = (gu32*)(ws + WS_EP);
      const unsigned tag = next_tag((unsigned)__builtin_amdgcn_readfirstlane(__hip_atomic_load(epw, RLX_AGENT)));
      gu32* f1 = (gu32*)(ws + WS_F1) + lane * 32;
      for (unsigned spins = 0; spins < (1u << 17); ++spins) {
        bool ok = true;
#pragma unroll
        for (int i = 0; i < 16; ++i) ok &= __hip_atomic_load(f1 + i, RLX_AGENT) == tag;
        if (__all(ok)) break;
        __builtin_amdgcn_s_sleep(2);
      }
      if (lane == 0) __hip_atomic_store(epw, tag, RLX_AGENT);
    }
    return;
  }

  __builtin_amdgcn_s_setprio(2);
  const int r = wid & 1, c = wid >> 1;
  STAMP(0);
  unsigned tag = 0;
  unsigned xa[8], wa[2][2][2], ka[8], va[4][2], lin = 0, fb = 0, fbw = 0;
  {
    const int lane = fresh_lane();
    if (wid == 0) { u32x2 z = {0u, 0u}; *(LAS u32x2*)(uintptr_t)(ldsb + MISC + lane * 8) = z; }
    wait_lgkm<0>();
    rawbar();
    const unsigned xb = (ldsb + rowbase(32 * r + (lane & 31), lane >> 5)) ^ (unsigned)((b & 1) * 128);
#pragma unroll
    for (int j = 0; j < 8; ++j) xa[j] = opaque(xb ^ (unsigned)(32 * j));
#pragma unroll
    for (int i = 0; i < 2; ++i)
#pragma unroll
      for (int t = 0; t < 2; ++t) { wa[i][t][0] = opaque(ldsb + trW(lane, 2 * c + i, t)); wa[i][t][1] = opaque(ldsb + trW(lane, 2 * c + i, t) + 8u * GS); }
    const unsigned kb = ldsb + rowbase(32 * c + (lane & 31), lane >> 5);
#pragma unroll
    for (int j = 0; j < 8; ++j) ka[j] = opaque(kb ^ (unsigned)(32 * (j ^ (4 * c))));
#pragma unroll
    for (int dt = 0; dt < 4; ++dt)
#pragma unroll
      for (int tt = 0; tt < 2; ++tt) va[dt][tt] = opaque(ldsb + trV(lane, dt ^ (2 * c), tt) + (unsigned)c * (8u * GS));
    lin = opaque(ldsb + lane * 16);
    fb = opaque(ldsb); fbw = opaque(ldsb + wid);
  }
  unsigned ctv = 0;
  { const int lane = fresh_lane(); if (wid == 0 && lane < CTL) ctv = *(const volatile GAS unsigned*)((pc0 & ~255ull) + (unsigned)((bid >> 3) * CTL + lane) * 64u); }
  const unsigned epv = __hip_atomic_load((gu32*)(ws + WS_EP), RLX_AGENT);
  const unsigned own4 = (unsigned)wid * (4 * GS), oth4 = (unsigned)(wid ^ 2) * (4 * GS);

  bf16x8 fr[192], fx[32], qf[8], pf[8][2];
  f32x16 qacc[2] = {}, sacc[8], oacc[4] = {};
  float pv[8][16];
  unsigned pw[8][8], cntv[NST];
  float ts[8], mref = 0.f, alph[8], lsum = 0.f;
  unsigned long long slow = 0;
  constexpr float CS = 0.088388347648318440f * 1.4426950408889634f;
  constexpr float THI = 1099511627776.0f, TLO = 1.0f / 1099511627776.0f;

  auto issue_reads = [&](auto N) {
    constexpr int n = N, t = n / 8;
    if constexpr (n < 64) {
      constexpr int ks = qs_ks(n), i = qs_i(n);
      constexpr unsigned so = soff(cu(t)) + (4 * (ks & 1)) * GS;
      if constexpr (i == 0) fx[t * 4 + ks] = ldsr16(xa[(t & 1) * 4 + ks] + soff(xu(t)));
      fr[n] = trread2(wa[i][0][ks >> 1] + so, wa[i][1][ks >> 1] + so);
    } else if constexpr (n < 128) {
      fr[n] = ldsr16(ka[n % 8] + soff(cu(t)));
    } else {
      constexpr unsigned so = soff(cu(t)) + (4 * pv_ks(n)) * GS;
      fr[n] = trread2(va[pv_dt(n)][0] + so, va[pv_dt(n)][1] + so);
    }
  };
  auto wait_stage = [&](auto T) {
    constexpr int t = T;
    if (__builtin_expect((unsigned)__builtin_amdgcn_readfirstlane(cntv[t]) != ALL4, 0)) spin_flag(fb + F_CNT + 4 * t, ALL4);
    cfence();
  };
  auto qpack = [&](int i) {
    f32x16 t = qacc[i] * CS;
#pragma unroll
    for (int s = 0; s < 2; ++s) { qf[2 * i + s] = pack8(t, s); ldsw16(lin + own4 + (unsigned)(2 * i + s) * GS + FRG, qf[2 * i + s]); }
  };
  auto vop = [&](auto K) {
    constexpr VOp o = VS.op[(int)K];
    constexpr int j = o.tile, e = o.idx;
    if constexpr (o.type == OP_CHK) {
      alph[j] = 1.0f;
      const bool bad = j == 0 ? !(ts[0] >= TLO && ts[0] <= THI) : !(ts[j] <= THI);
      if (__builtin_expect((slow | __builtin_amdgcn_ballot_w64(bad)) != 0, 0)) {
        float m = sacc[j][0];
#pragma unroll
        for (int i = 1; i < 16; ++i) m = fmaxf(m, sacc[j][i]);
        m = swapmax(m);
        const float mn = j == 0 ? m : fmaxf(mref, m);
        if constexpr (j > 0) { alph[j] = __builtin_amdgcn_exp2f(mref - mn); lsum *= alph[j]; }
        mref = mn; slow = 1;
        f32x16 p; float sum = 0.f;
#pragma unroll
        for (int i = 0; i < 16; ++i) { p[i] = __builtin_amdgcn_exp2f(sacc[j][i] - mn); sum += p[i]; }
        ts[j] = sum; pf[j][0] = pack8(p, 0); pf[j][1] = pack8(p, 1);
      }
      lsum = pinf(lsum + ts[j]);
    } else if constexpr (o.type == OP_EX) {
      pv[j][e] = pinf(__builtin_amdgcn_exp2f(sacc[j][e]));
    } else if constexpr (o.type == OP_ADD) {
      if constexpr (e == 1) ts[j] = pinf(pv[j][0] + pv[j][1]); else ts[j] = pinf(ts[j] + pv[j][e]);
    } else {
      pw[j][e] = opaque(pk2(pv[j][2 * e], pv[j][2 * e + 1]));
      if constexpr ((e & 3) == 3) { u32x4 p = {pw[j][e - 3], pw[j][e - 2], pw[j][e - 1], pw[j][e]}; pf[j][e >> 2] = __builtin_bit_cast(bf16x8, p); }
    }
  };
  auto osend = [&](int i) {
#pragma unroll
    for (int s = 0; s < 2; ++s) ldsw16(lin + own4 + exc(i * 2 + s), pack8(oacc[2 + i], s));
  };
  float lw = 0.f;
  unsigned relq = 0;
#define CST(i)
  unsigned cst[32];

  spin_flag(fb + F_CNT, ALL4);
  STAMP(1);
  static_for<WLA>([&](auto N) { issue_reads(N); });
  SB();
  static_for<192>([&](auto N) {
    constexpr int n = N, t = n / 8;
    if constexpr (n >= 136 && n % 8 == 0) {
      if (__builtin_expect(slow != 0, 0)) {
#pragma unroll
        for (int dt = 0; dt < 4; ++dt) oacc[dt] *= alph[t - 16];
      }
    }
    if constexpr (n % 8 == 0) { if constexpr (n % 32 == 0) { CST(t); } else cst[t] = cst[t - 1]; }
    if constexpr (n < 64) {
      constexpr int ks = qs_ks(n), i = qs_i(n);
      qacc[i] = MFMA32(fr[n], fx[t * 4 + ks], qacc[i]);
    } else if constexpr (n < 128) {
      constexpr int j = t - 8, k = n % 8;
      if constexpr (k == 0) { f32x16 z = {}; sacc[j] = MFMA32(fr[n], qf[0], z); }
      else sacc[j] = MFMA32(fr[n], qf[k], sacc[j]);
    } else {
      oacc[pv_dt(n)] = MFMA32(fr[n], pf[t - 16][pv_ks(n)], oacc[pv_dt(n)]);
    }
    SB();
    if constexpr (n == 48) {
      tag = next_tag((unsigned)__builtin_amdgcn_readfirstlane(epv));
      asm volatile("" :: "v"(ctv));
    }
    static_for<3>([&](auto I) {
      constexpr int m = first_read(n) + (int)I;
      if constexpr (m < 192 && n != 63) {
        if constexpr (isl(m) == n) {
          if constexpr (m % 8 == 0) wait_stage(std::integral_constant<int, m / 8>{});
          issue_reads(std::integral_constant<int, m>{});
          if constexpr (m % 8 == 7) { if constexpr (PL.rel[m / 8]) lds_set(fbw + F_REL + 4 * (m / 8)); }
        }
      }
    });
    static_for<NST>([&](auto T) { constexpr int tt = T; if constexpr (tt > 0 && tt != 8 && fpre(tt) == n) cntv[tt] = ldsflag(fb + F_CNT + 4 * tt); });
    if constexpr (n == 55 && PL.qrel >= 0) relq = ldsflag(fb + F_REL + 4 * PL.qrel);
    if constexpr (n == 61) {
      if constexpr (PL.qrel >= 0) { if (__builtin_expect((unsigned)__builtin_amdgcn_readfirstlane(relq) != ALL4, 0)) spin_flag(fb + F_REL + 4 * PL.qrel, ALL4); }
      cfence();
      qpack(0);
    }
    if constexpr (n == 184) relq = ldsflag(fb + F_REL + 4 * PL.erel);
    if constexpr (n == 63) {
      qpack(1);
      lds_set(fb + F_Q + 4 * wid);
      CST(26);
      spin_flag(fb + F_Q + 4 * (wid ^ 2), 1u);
      CST(27);
#pragma unroll
      for (int k = 0; k < 4; ++k) qf[4 + k] = ldsr16(lin + oth4 + (unsigned)k * GS + FRG);
      spin_flag(fb + F_CNT + 4 * 8, ALL4);
      static_for<WLA>([&](auto I) { issue_reads(std::integral_constant<int, 64 + (int)I>{}); });
    }
    if constexpr (n >= 64) static_for<VS.start[n + 1] - VS.start[n]>([&](auto K) { vop(std::integral_constant<int, VS.start[n] + (int)K>{}); });
    if constexpr (n == 188) {
      lw = swapadd(lsum);
      f32x2 st = {mref, lw};
      *(LAS f32x2*)(uintptr_t)(lin + (unsigned)(1 + wid) * GS + MISC) = st;
    }
    if constexpr (n == 189) {
      if (__builtin_expect((unsigned)__builtin_amdgcn_readfirstlane(relq) != ALL4, 0)) spin_flag(fb + F_REL + 4 * PL.erel, ALL4);
      cfence();
      osend(0);
    }
    if constexpr (n == 190) { osend(1); lds_set(fb + F_O1 + 4 * wid); }
    SB();
  });
  STAMP(30);
  CST(24);
  spin_flag(fb + F_O1 + 4 * (wid ^ 2), 1u);
  STAMP(31);
  const int lane = fresh_lane();
  const __amdgpu_buffer_rsrc_t wsr = __builtin_amdgcn_make_buffer_rsrc(ws, 0, 16u << 20, 0x00020000);
  bf16x8 of[4][4];
  {
    const f32x2 so2 = *(const LAS f32x2*)(uintptr_t)(lin + (unsigned)(1 + (wid ^ 2)) * GS + MISC);
    u32x4 v[4];
#pragma unroll
    for (int k = 0; k < 4; ++k) v[k] = *(const LAS u32x4*)(uintptr_t)(lin + oth4 + exc(k));
    const float M = fmaxf(mref, so2[0]);
    const float bs = __builtin_amdgcn_exp2f(mref - M), bo = __builtin_amdgcn_exp2f(so2[0] - M);
    const float inv = 1.0f / (lw * bs + so2[1] * bo);
    const float fs = bs * inv, fo2 = bo * inv;
    const unsigned goff = WS_O + (unsigned)(((bid * 2 + r) * 4 + 2 * c) * 2) * 1024 + lane * 16;
#pragma unroll
    for (int i = 0; i < 2; ++i) {
      f32x16 t;
#pragma unroll
      for (int e = 0; e < 16; ++e) {
        const unsigned w = v[i * 2 + (e >> 3)][(e & 7) >> 1];
        t[e] = fmaf(__uint_as_float((e & 1) ? (w & 0xffff0000u) : (w << 16)), fo2, oacc[i][e] * fs);
      }
#pragma unroll
      for (int s = 0; s < 2; ++s) {
        of[0][i * 2 + s] = pack8(t, s);
        __builtin_amdgcn_raw_buffer_store_b128(__builtin_bit_cast(u32x4, of[0][i * 2 + s]), wsr, goff + (i * 2 + s) * 1024, 0, 16);
      }
    }
    cfence();
  }
  unsigned ob[4][2], oa[2][4][2][2];
#pragma unroll
  for (int k = 0; k < 4; ++k)
#pragma unroll
    for (int tt = 0; tt < 2; ++tt) ob[k][tt] = ldsb + trV(lane, k ^ (2 * c), tt);
  auto oaddr = [&](auto JJ) {
    constexpr int jj = JJ;
    const unsigned so = c ? soff(28 + 2 * jj + 1) : soff(28 + 2 * jj);
#pragma unroll
    for (int k = 0; k < 4; ++k)
#pragma unroll
      for (int tt = 0; tt < 2; ++tt) { oa[jj & 1][k][tt][0] = opaque(ob[k][tt] + so); oa[jj & 1][k][tt][1] = opaque(ob[k][tt] + so + 8u * GS); }
  };
  bf16x8 fo[64];
  f32x16 pacc[4] = {};
  auto oreads = [&](auto M) {
    constexpr int m = M, ks4 = op_ks(m), k = op_ct(m), jj = m / 16;
    constexpr unsigned so = (4 * (ks4 & 1)) * GS;
    if constexpr (m % 16 == 0) { spin_flag(fb + F_WO + 4 * jj, ALL4); oaddr(std::integral_constant<int, jj>{}); }
    fo[m] = trread2(oa[jj & 1][k][0][ks4 >> 1] + so, oa[jj & 1][k][1][ks4 >> 1] + so);
    if constexpr (m == 15) lds_set(fbw + F_O2);
  };
  STAMP(32);
  asm volatile("s_waitcnt vmcnt(0)" ::: "memory");
  if (lane == 0) __hip_atomic_store((gu32*)(ws + WS_F1) + (b * 8 + g) * 32 + hh * 4 + wid, tag, RLX_AGENT);
  STAMP(33);
  static_for<OLA0>([&](auto M) { oreads(M); });
  STAMP(34);
  {
    gu32* f1 = (gu32*)(ws + WS_F1) + (b * 8 + g) * 32 + ((hh + 1 + min(lane, 2)) & 3) * 4 + wid;
    for (unsigned spins = 0; spins < (1u << 17); ++spins) {
      if (__all(__hip_atomic_load(f1, RLX_AGENT) == tag)) break;
    }
    asm volatile("" ::: "memory");
    STAMP(35);
#pragma unroll
    for (int j2 = 1; j2 < 4; ++j2) {
      const int sb = (bid & ~24) | (((hh + j2) & 3) << 3);
#pragma unroll
      for (int k = 0; k < 4; ++k)
        of[j2][k] = __builtin_bit_cast(bf16x8, __builtin_amdgcn_raw_buffer_load_b128(wsr, WS_O + (unsigned)((sb * 2 + r) * 8 + 4 * c + k) * 1024 + lane * 16, 0, 16));
    }
  }
  const unsigned xo = lin + (unsigned)r * (8 * GS) + (c ? soff(29) : soff(28)), xi = lin + (unsigned)r * (8 * GS) + (c ? soff(28) : soff(29));
  const unsigned xo0 = opaque(xo), xo1 = opaque(xo + 4 * GS), xi0 = opaque(xi), xi1 = opaque(xi + 4 * GS);
  unsigned relo = 0;
  SB();
  static_for<64>([&](auto M) {
    constexpr int m = M, jj = m / 16;
    pacc[op_ct(m)] = MFMA32(fo[m], of[jj][op_ks(m)], pacc[op_ct(m)]);
    SB();
    static_for<2>([&](auto I) {
      constexpr int mm = op_first(m) + (int)I;
      if constexpr (mm < 64) { if constexpr (op_isl(mm) == m) oreads(std::integral_constant<int, mm>{}); }
    });
    if constexpr (m == 50) relo = ldsflag(fb + F_O2);
    if constexpr (m == 57) {
      if (__builtin_expect((unsigned)__builtin_amdgcn_readfirstlane(relo) != ALL4, 0)) spin_flag(fb + F_O2, ALL4);
      cfence();
#pragma unroll
      for (int gq = 0; gq < 4; ++gq) { f32x4 v = {pacc[2][4 * gq], pacc[2][4 * gq + 1], pacc[2][4 * gq + 2], pacc[2][4 * gq + 3]}; *(LAS f32x4*)(uintptr_t)(xo0 + (unsigned)gq * GS) = v; }
    }
    if constexpr (m == 58) {
#pragma unroll
      for (int gq = 0; gq < 4; ++gq) { f32x4 v = {pacc[3][4 * gq], pacc[3][4 * gq + 1], pacc[3][4 * gq + 2], pacc[3][4 * gq + 3]}; *(LAS f32x4*)(uintptr_t)(xo1 + (unsigned)gq * GS) = v; }
      lds_set(fb + F_P + 4 * wid);
    }
    SB();
  });
  CST(25);
  STAMP(36);
  spin_flag(fb + F_P + 4 * (wid ^ 2), 1u);
  {
    f32x4 v[8];
#pragma unroll
    for (int k = 0; k < 8; ++k) v[k] = *(const LAS f32x4*)(uintptr_t)((k < 4 ? xi0 : xi1) + (unsigned)(k & 3) * GS);
#pragma unroll
    for (int ci = 0; ci < 2; ++ci) {
      const int cons = (bid & ~7) | (2 * (2 * c + ci) + r);
      f32x16 t;
#pragma unroll
      for (int e = 0; e < 16; ++e) t[e] = pacc[ci][e] + v[ci * 4 + (e >> 2)][e & 3];
#pragma unroll
      for (int s = 0; s < 2; ++s)
        __builtin_amdgcn_raw_buffer_store_b128(__builtin_bit_cast(u32x4, pack8(t, s)), wsr, WS_P + (unsigned)((cons * 8 + g) * 2 + s) * 1024 + lane * 16, 0, 16);
    }
  }
  asm volatile("s_waitcnt vmcnt(0)" ::: "memory");
  if (lane < 2) {
    const int cons = (bid & ~7) | (2 * (2 * c + lane) + r);
    __hip_atomic_store((gu32*)(ws + WS_F2) + cons * 32 + g, tag, RLX_AGENT);
  }
  STAMP(37);
  {
    gu32* f2 = (gu32*)(ws + WS_F2) + bid * 32 + (lane & 7);
    const unsigned poff = WS_P + (unsigned)((bid * 8 + (lane >> 5) * 4) * 2 + (wid & 1)) * 1024 + ((wid >> 1) * 32 + (lane & 31)) * 16;
    for (unsigned spins = 0; spins < (1u << 17); ++spins) {
      if (__all(__hip_atomic_load(f2, RLX_AGENT) == tag)) break;
    }
    asm volatile("" ::: "memory");
    STAMP(38);
    u32x4 pp[4];
#pragma unroll
    for (int p = 0; p < 4; ++p) pp[p] = __builtin_amdgcn_raw_buffer_load_b128(wsr, poff + p * 2048, 0, 16);
    float sm[8];
#pragma unroll
    for (int e = 0; e < 8; ++e) {
      float q4[4];
#pragma unroll
      for (int p = 0; p < 4; ++p) { const unsigned w = pp[p][e >> 1]; q4[p] = __uint_as_float((e & 1) ? (w & 0xffff0000u) : (w << 16)); }
      sm[e] = (q4[0] + q4[1]) + (q4[2] + q4[3]);
    }
    float s[4];
#pragma unroll
    for (int e = 0; e < 4; ++e) {
      auto rr = __builtin_amdgcn_permlane32_swap(__float_as_uint(sm[e]), __float_as_uint(sm[4 + e]), false, false);
      s[e] = __uint_as_float(rr[0]) + __uint_as_float(rr[1]);
    }
    const int ct = g >> 1, rr = g & 1;
    u32x2 o2 = {pk2(s[0], s[1]), pk2(s[2], s[3])};
    *(u32x2*)(out + (size_t)(b * 64 + rr * 32 + (lane & 31)) * 512 + hh * 128 + ct * 32 + 8 * (2 * (wid & 1) + (lane >> 5)) + 4 * (wid >> 1)) = o2;
  }
  STAMP(39);
}

extern "C" void kernel_launch(void* const* d_in, const int* in_sizes, int n_in, void* d_out, int out_size, void* d_ws, size_t ws_size, hipStream_t stream) {
  (void)hipFuncSetAttribute((const void*)gqa_kernel, hipFuncAttributeMaxDynamicSharedMemorySize, LDSB);
  gqa_kernel<<<256, 512, LDSB, stream>>>((const u16*)d_in[0], (const u16*)d_in[1], (const u16*)d_in[2], (const u16*)d_in[3], (const u16*)d_in[4], (char*)d_ws, (u16*)d_out);
}
```

```cpp
#include <hip/hip_runtime.h>
#include <hip/hip_bf16.h>
#include <utility>

typedef unsigned short u16;
typedef __bf16 bf16x8 __attribute__((ext_vector_type(8)));
typedef __bf16 bf16x2 __attribute__((ext_vector_type(2)));
typedef short s16x4 __attribute__((ext_vector_type(4)));
typedef float f32x16 __attribute__((ext_vector_type(16)));
typedef float f32x4 __attribute__((ext_vector_type(4)));
typedef float f32x2 __attribute__((ext_vector_type(2)));
typedef unsigned u32x4 __attribute__((ext_vector_type(4)));
typedef unsigned u32x2 __attribute__((ext_vector_type(2)));
#define LAS __attribute__((address_space(3)))
#define GAS __attribute__((address_space(1)))
typedef GAS unsigned gu32;
#define DI __device__ __forceinline__
#define MFMA32(a, b, c) __builtin_amdgcn_mfma_f32_32x32x16_bf16((a), (b), (c), 0, 0, 0)
#define RLX_AGENT __ATOMIC_RELAXED, __HIP_MEMORY_SCOPE_AGENT
#define SB() __builtin_amdgcn_sched_barrier(0)

DI unsigned pk2(float a, float b) { f32x2 v = {a, b}; bf16x2 r = __builtin_convertvector(v, bf16x2); return __builtin_bit_cast(unsigned, r); }
DI bf16x8 pack8(const f32x16& x, int s) {
  u32x4 p = {pk2(x[8 * s], x[8 * s + 1]), pk2(x[8 * s + 2], x[8 * s + 3]), pk2(x[8 * s + 4], x[8 * s + 5]), pk2(x[8 * s + 6], x[8 * s + 7])};
  return __builtin_bit_cast(bf16x8, p);
}
DI bf16x8 trread2(unsigned a0, unsigned a1) {
  s16x4 lo = __builtin_amdgcn_ds_read_tr16_b64_v4i16((LAS s16x4*)(uintptr_t)a0);
  s16x4 hi = __builtin_amdgcn_ds_read_tr16_b64_v4i16((LAS s16x4*)(uintptr_t)a1);
  return __builtin_bit_cast(bf16x8, __builtin_shufflevector(lo, hi, 0, 1, 2, 3, 4, 5, 6, 7));
}
DI bf16x8 ldsr16(unsigned a) { return *(const LAS bf16x8*)(uintptr_t)a; }
DI void ldsw16(unsigned a, bf16x8 v) { *(LAS bf16x8*)(uintptr_t)a = v; }
DI void glds16(const void* sbase, unsigned voff, unsigned lds_dst) {
  unsigned keep;
  asm volatile("s_mov_b32 %0, m0\n\ts_mov_b32 m0, %3\n\ts_nop 0\n\tglobal_load_lds_dwordx4 %1, %2\n\ts_mov_b32 m0, %0"
               : "=&s"(keep) : "v"(voff), "s"(sbase), "s"(lds_dst) : "memory");
}
template <int N> DI void wait_vm() { asm volatile("s_waitcnt vmcnt(%0)" ::"n"(N) : "memory"); }
template <int N> DI void wait_lgkm() { asm volatile("s_waitcnt lgkmcnt(%0)" ::"n"(N) : "memory"); }
DI void rawbar() { asm volatile("" ::: "memory"); __builtin_amdgcn_s_barrier(); asm volatile("" ::: "memory"); }
DI float swapmax(float m) { auto rr = __builtin_amdgcn_permlane32_swap(__float_as_uint(m), __float_as_uint(m), false, false); return fmaxf(__uint_as_float(rr[0]), __uint_as_float(rr[1])); }
DI float swapadd(float m) { auto rr = __builtin_amdgcn_permlane32_swap(__float_as_uint(m), __float_as_uint(m), false, false); return __uint_as_float(rr[0]) + __uint_as_float(rr[1]); }
DI unsigned opaque(unsigned v) { asm volatile("" : "+v"(v)); return v; }
DI float pinf(float v) { asm volatile("" : "+v"(v)); return v; }
DI float max3(float a, float b, float c) { float r; asm("v_max3_f32 %0, %1, %2, %3" : "=v"(r) : "v"(a), "v"(b), "v"(c)); return r; }
DI int fresh_lane() { int l; asm volatile("v_mbcnt_lo_u32_b32 %0, -1, 0\n\tv_mbcnt_hi_u32_b32 %0, -1, %0" : "=v"(l)); return l; }

template <typename F, int... I> DI void static_for_impl(F&& f, std::integer_sequence<int, I...>) { (f(std::integral_constant<int, I>{}), ...); }
template <int N, typename F> DI void static_for(F&& f) { static_for_impl(f, std::make_integer_sequence<int, N>{}); }

constexpr int NSLOT = 10, GS = NSLOT * 1024, LDSB = 16 * GS, NUNITS = 36, NST = 24;
DI int swz(int R) { return ((R & 3) << 2) | ((R >> 2) & 3); }
DI unsigned rowbase(int R, int hf) { return (unsigned)((R >> 2) * GS + (R & 3) * 256 + 16 * (hf ^ swz(R))); }
DI unsigned trW(int lane, int dt, int t) {
  const int hf = lane >> 5, q4 = (lane & 15) >> 2, p4 = lane & 3, blk = (lane >> 4) & 1;
  const int pc = ((p4 & 1) << 1) | (p4 >> 1);
  return (unsigned)((2 * hf + t) * GS + q4 * 256 + 16 * (((dt ^ q4) << 2) | ((2 * blk + (pc >> 1)) ^ (2 * hf + t))) + 8 * (pc & 1));
}
DI unsigned trV(int lane, int dt, int t) {
  const int hf = lane >> 5, q4 = (lane & 15) >> 2, p4 = lane & 3, blk = (lane >> 4) & 1;
  return (unsigned)((2 * t + hf) * GS + q4 * 256 + 16 * (((dt ^ q4) << 2) | ((2 * blk + (p4 >> 1)) ^ (2 * t + hf))) + 8 * (p4 & 1));
}

#define WLA 4
#define LDEPTH 3
#define TK -1
#define TV -1
#define TW 27
constexpr int cu(int t) { return t < 8 ? (t / 2) * 3 + 1 + (t & 1) : t + 4; }
constexpr int xu(int t) { return (t / 2) * 3; }
constexpr int stage_of(int u) { for (int t = 0; t < NST; ++t) if (cu(t) == u) return t; return -1; }
#define VPOS 8
#define EPOS 34
#define RELMASK 0x1ffffff
#define QPOS 15
struct Plan { int order[NUNITS], slot[NUNITS], need[NUNITS], qslot, qrel, e0, e1, erel; bool rel[26]; };
constexpr int last_use(int u) { return u < 12 ? (u % 3 == 0 ? (u / 3) * 2 + 1 : stage_of(u)) : (u < 28 ? u - 4 : 99); }
constexpr Plan make_plan() {
  Plan P{};
  int n = 0;
  for (int u = 0; u < 12 + VPOS; ++u) P.order[n++] = u;
  P.order[n++] = 20; P.order[n++] = 21;
  for (int u = 12 + VPOS; u < 20; ++u) P.order[n++] = u;
  for (int u = 22; u < 36; ++u) P.order[n++] = u;
  int fr[9] = {-1, -1, -1, -1, -1, -1, -1, -1, -1};
  auto take = [&](int until) { int bs = 0; for (int k = 1; k < 9; ++k) if (fr[k] < fr[bs]) bs = k; int f = fr[bs]; fr[bs] = until; return bs * 100 + (f + 1); };
  int run = -1;
  for (int p = 0; p < NUNITS; ++p) {
    if (p == QPOS) { int t = take(8); P.qslot = t / 100; P.qrel = t % 100 - 1; }
    if (p == EPOS) { int t = take(99); P.e0 = t / 100; P.erel = t % 100 - 1; }
    const int u = P.order[p];
    int t = take(last_use(u));
    P.slot[u] = t / 100;
    int f = t % 100 - 1;
    while (f >= 0 && !((RELMASK >> f) & 1)) ++f;
    if (f > run) { run = f; P.need[p] = f; P.rel[f] = true; } else P.need[p] = -1;
  }
  if (P.qrel >= 0) P.rel[P.qrel] = true;
  P.rel[P.erel] = true;
  return P;
}
constexpr Plan PL = make_plan();
static_assert(PL.order[0] == 0 && PL.need[0] < 0 && PL.need[1] < 0 && PL.need[2] < 0 && PL.need[3] < 0 && PL.need[4] < 0 && PL.need[5] < 0 && LDEPTH <= 4, "no flag access before the barrier");
static_assert(PL.qrel <= 6 && PL.erel <= 22, "plan");
constexpr int ntouch(int p) { return (p == TK ? 2 : 0) + (p == TV ? 2 : 0) + (p == TW ? 1 : 0); }
constexpr int ntouch_in(int q, int last) { int n = 0; for (int p = q; p <= last; ++p) n += ntouch(p); return n; }
constexpr unsigned soff(int u) { return (unsigned)PL.slot[u] * 1024u; }
constexpr unsigned exc(int k) { return (unsigned)(k * GS + PL.e0 * 1024); }
constexpr unsigned FRG = PL.qslot * 1024, MISC = 9216;
constexpr unsigned F_CNT = MISC, F_WO = MISC + 96, F_Q = MISC + 112, F_O1 = MISC + 128, F_O2 = MISC + 144, F_REL = MISC + 160, F_P = MISC + 288;

#define FSTR 32
constexpr unsigned WS_O = 0, WS_P = 4u << 20, WS_F1 = 12u << 20, WS_F2 = WS_F1 + 64 * FSTR * 4 + 65536, WS_EP = WS_F2 + 256 * FSTR * 4 + 65536;
static_assert(WS_EP + 1024 <= (16u << 20), "workspace layout");

DI unsigned next_tag(unsigned e) { return e * 0x9E3779B1u + 0x7F4A7C15u; }
#define LOADER_FIRST(x) __builtin_expect((x), 1)
DI unsigned ldsflag(unsigned a) { return *(volatile const LAS unsigned*)(uintptr_t)a; }
DI void cfence() { asm volatile("" ::: "memory"); }
DI void spin_flag(unsigned a, unsigned want) {
  for (unsigned s = 0; s < (1u << 22); ++s) { if ((unsigned)__builtin_amdgcn_readfirstlane(ldsflag(a)) == want) break; }
  cfence();
}
#define LSLEEP 1
DI void spin_flag_slow(unsigned a, unsigned want) {
  for (unsigned s = 0; s < (1u << 20); ++s) { if ((unsigned)__builtin_amdgcn_readfirstlane(ldsflag(a)) == want) break; if (LSLEEP) __builtin_amdgcn_s_sleep(LSLEEP); }
  cfence();
}
constexpr unsigned ALL4 = 0x01010101u;
DI void lds_set(unsigned a) { cfence(); *(volatile LAS unsigned char*)(uintptr_t)a = 1; cfence(); }

#define WLA2 6
constexpr int isl(int m) { return m < 64 ? m - WLA : (m < 64 + WLA ? 63 : (64 + (m - 64 - WLA) / 2 > m - WLA2 ? 64 + (m - 64 - WLA) / 2 : m - WLA2)); }
#define FPRE 5
constexpr int fpre(int t) { int n = isl(8 * t) - FPRE; if (t > 8 && n < 64) n = 64; return n < 0 ? 0 : n; }
constexpr int first_read(int n) { for (int m = 0; m < 192; ++m) if (isl(m) >= n) return m; return 192; }
constexpr int qs_ks(int n) { return n < 56 ? (n % 8) / 2 : (n % 4); }
constexpr int qs_i(int n) { return n < 56 ? n % 2 : (n % 8) / 4; }
constexpr int pv_ks(int n) { return n < 184 ? (n % 8) / 4 : (n & 3) >> 1; }
constexpr int pv_dt(int n) { return n < 184 ? n % 4 : (n < 188 ? 2 : 0) + (n & 1); }

constexpr int op_ks(int m) { return m < 48 ? (m % 16) / 4 : (m % 8) / 2; }
constexpr int op_ct(int m) { return m < 48 ? m % 4 : (m < 56 ? 2 : 0) + (m & 1); }
#define OLA0 8
#define OLA 12
constexpr int op_isl(int m) { return m < OLA0 ? -1 : ((m - OLA0) / 2 > m - OLA ? (m - OLA0) / 2 : m - OLA); }
constexpr int op_first(int n) { for (int m = OLA0; m < 64; ++m) if (op_isl(m) >= n) return m; return 64; }
enum { OP_CHK, OP_EX, OP_ADD, OP_CVT };
struct VOp { int type, tile, idx; };
struct VSched { VOp op[512]; int start[194]; };
#define BQK 25
#define BPV 20
constexpr VSched make_sched() {
  VSched s{};
  VOp ex[48] = {};
  int k = 0;
  for (int e = 0; e < 16; ++e) {
    ex[k++] = VOp{OP_EX, 0, e};
    if (e >= 2) ex[k++] = VOp{OP_ADD, 0, e - 1};
    if (e >= 2 && (e & 1) == 0) ex[k++] = VOp{OP_CVT, 0, (e - 2) / 2};
  }
  ex[k++] = VOp{OP_ADD, 0, 15};
  ex[k++] = VOp{OP_CVT, 0, 7};
  ex[k++] = VOp{OP_CHK, 0, 0};
  const int len = k;
  int je = 0, ke = 0, no = 0, carry = 0;
  for (int n = 0; n < 192; ++n) {
    s.start[n] = no;
    if (n < 64) continue;
    int bud = (n < 128 ? BQK : BPV) + carry;
    while (je < 8 && n >= 64 + 8 * je + 7 + 2) {
      VOp o = ex[ke]; o.tile = je;
      int cost = o.type == OP_EX ? 11 : (o.type == OP_CHK ? 14 : 5);
      int dl = 128 + 8 * je - 2 - (len - 1 - ke) / 4;
      if (bud < cost && n < dl) break;
      s.op[no++] = o; bud -= cost;
      if (++ke == len) { ++je; ke = 0; }
    }
    carry = bud > 6 ? 6 : (bud < -6 ? -6 : bud);
  }
  s.start[192] = no; s.start[193] = je == 8 ? 1 : 0;
  return s;
}
constexpr VSched VS = make_sched();
static_assert(VS.start[193] == 1, "schedule incomplete");

#define STAMP(i)

__global__ __launch_bounds__(512, 2) void gqa_kernel(const u16* __restrict__ x, const u16* __restrict__ kc, const u16* __restrict__ vc,
                                                     const u16* __restrict__ wq, const u16* __restrict__ wo, char* __restrict__ ws, u16* __restrict__ out) {
  extern __shared__ __attribute__((aligned(16))) char lds[];
  const int wid8 = __builtin_amdgcn_readfirstlane(threadIdx.x >> 6);
  const int wid = wid8 & 3;
  const int bid = blockIdx.x;
  const int g = bid & 7, b = bid >> 5, hh = (bid >> 3) & 3, h = g * 4 + hh;
  const unsigned ldsb = (unsigned)(uintptr_t)(LAS char*)lds;

  if (LOADER_FIRST(wid8 >= 4)) {
    const int lane = fresh_lane();
    unsigned voffx[4], voffw[4], voffk[4];
#pragma unroll
    for (int i = 0; i < 4; ++i) {
      unsigned row = wid * 16 + (lane >> 4) + 4 * i;
      unsigned ch = (lane & 15) ^ (((lane >> 4) << 2) | i);
      voffx[i] = row * 1024 + ch * 16; voffw[i] = row * 8192 + ch * 16; voffk[i] = row * 256 + ch * 16;
    }
    auto issue = [&](auto U) {
      constexpr int u = U;
      const unsigned dst = ldsb + soff(u) + wid * (4 * GS);
      if constexpr (u < 12) {
        const int q = ((u / 3) + (b >> 1) + hh) & 3; constexpr int rr = u % 3;
        if constexpr (rr == 0) { const u16* src = x + (size_t)b * 64 * 512 + q * 128;
#pragma unroll
          for (int i = 0; i < 4; ++i) glds16(src, voffx[i], dst + i * GS); }
        else { int wu = q * 2 + ((rr - 1) ^ (b & 1)); const u16* src = wq + (size_t)wu * 64 * 4096 + h * 128;
#pragma unroll
          for (int i = 0; i < 4; ++i) glds16(src, voffw[i], dst + i * GS); }
      } else if constexpr (u < 28) {
        int k = (u - 12 + 2 * hh) & 7; const u16* src = (u < 20 ? kc : vc) + ((size_t)(b * 8 + g) * 512 + k * 64) * 128;
#pragma unroll
        for (int i = 0; i < 4; ++i) glds16(src, voffk[i], dst + i * GS);
      } else {
        int k = (u - 28 + 2 * hh) & 7; const u16* src = wo + ((size_t)g * 512 + k * 64) * 512 + hh * 128;
#pragma unroll
        for (int i = 0; i < 4; ++i) glds16(src, voffx[i], dst + i * GS);
      }
    };
#define LST(i)
    const unsigned fb = opaque(ldsb), fbw = opaque(ldsb + wid);
    auto signal = [&](auto Q) {
      constexpr int u = PL.order[(int)Q];
      if constexpr (u < 28) { if constexpr (stage_of(u) >= 0) { lds_set(fbw + F_CNT + 4 * stage_of(u)); LST(stage_of(u)); } }
      else if constexpr (u & 1) { lds_set(fbw + F_WO + 4 * ((u - 28) >> 1)); LST(25 + ((u - 28) >> 1)); }
    };
    unsigned tch[5] = {0, 0, 0, 0, 0};
    auto post = [&](auto P) {
      constexpr int p = P;
      issue(std::integral_constant<int, PL.order[p]>{});
      if constexpr (p == TK) {
        const char* tp = (const char*)(kc + ((size_t)(b * 8 + g) * 512 + hh * 128) * 128) + (wid * 64 + lane) * 128;
        asm volatile("global_load_dword %0, %1, off" : "=v"(tch[0]) : "v"(tp) : "memory");
        asm volatile("global_load_dword %0, %1, off offset:64" : "=v"(tch[3]) : "v"(tp) : "memory");
      }
      if constexpr (p == TV) {
        const char* tp = (const char*)(vc + ((size_t)(b * 8 + g) * 512 + hh * 128) * 128) + (wid * 64 + lane) * 128;
        asm volatile("global_load_dword %0, %1, off" : "=v"(tch[1]) : "v"(tp) : "memory");
        asm volatile("global_load_dword %0, %1, off offset:64" : "=v"(tch[4]) : "v"(tp) : "memory");
      }
      if constexpr (p == TW) {
        const char* tp = (const char*)(wo + (size_t)g * 512 * 512) + (bid >> 3) * 16384 + (wid * 64 + lane) * 64;
        asm volatile("global_load_dword %0, %1, off" : "=v"(tch[2]) : "v"(tp) : "memory");
      }
    };
    LST(31);
    static_for<LDEPTH - 1>([&](auto P) { post(P); });
    int sdone = -1;
    unsigned relv = 0;
    static_for<NUNITS>([&](auto Q) {
      constexpr int q = Q, p = q + LDEPTH - 1;
      if constexpr (p < NUNITS) {
        constexpr int k = PL.need[p];
        if constexpr (k >= 0) {
          if ((unsigned)__builtin_amdgcn_readfirstlane(relv) != ALL4) {
            static_for<LDEPTH - 1>([&](auto I) {
              constexpr int qq = q + (int)I;
              wait_vm<(p - 1 - qq) * 4 + ntouch_in(qq, p - 1)>();
              if (qq > sdone) signal(std::integral_constant<int, qq>{});
            });
            sdone = p - 1;
            spin_flag_slow(fb + F_REL + 4 * k, ALL4);
          }
          cfence();
        }
        if constexpr (p + 1 < NUNITS) { constexpr int k1 = PL.need[p + 1]; if constexpr (k1 >= 0) relv = ldsflag(fb + F_REL + 4 * k1); }
        post(std::integral_constant<int, p>{});
      }
      constexpr int last = p < NUNITS ? p : NUNITS - 1;
      wait_vm<(last - q) * 4 + ntouch_in(q, last)>();
      if constexpr (q == 1) rawbar();
      if (q > sdone) signal(Q);
    });
    wait_vm<0>();
    asm volatile("" :: "v"(tch[0]), "v"(tch[1]), "v"(tch[2]), "v"(tch[3]), "v"(tch[4]));
    return;
  }

  __builtin_amdgcn_s_setprio(2);
  const int r = wid & 1, c = wid >> 1;
  STAMP(0);
  unsigned tag = 0;
  unsigned xa[8], wa[2][2][2], ka[8], va[4][2], lin = 0, fb = 0, fbw = 0;
  {
    const int lane = fresh_lane();
    if (wid == 0) { u32x2 z = {0u, 0u}; *(LAS u32x2*)(uintptr_t)(ldsb + MISC + lane * 8) = z; }
    wait_lgkm<0>();
    rawbar();
    const unsigned xb = (ldsb + rowbase(32 * r + (lane & 31), lane >> 5)) ^ (unsigned)((b & 1) * 128);
#pragma unroll
    for (int j = 0; j < 8; ++j) xa[j] = opaque(xb ^ (unsigned)(32 * j));
#pragma unroll
    for (int i = 0; i < 2; ++i)
#pragma unroll
      for (int t = 0; t < 2; ++t) { wa[i][t][0] = opaque(ldsb + trW(lane, 2 * c + i, t)); wa[i][t][1] = opaque(ldsb + trW(lane, 2 * c + i, t) + 8u * GS); }
    const unsigned kb = ldsb + rowbase(32 * c + (lane & 31), lane >> 5);
#pragma unroll
    for (int j = 0; j < 8; ++j) ka[j] = opaque(kb ^ (unsigned)(32 * (j ^ (4 * c))));
#pragma unroll
    for (int dt = 0; dt < 4; ++dt)
#pragma unroll
      for (int tt = 0; tt < 2; ++tt) va[dt][tt] = opaque(ldsb + trV(lane, dt ^ (2 * c), tt) + (unsigned)c * (8u * GS));
    lin = opaque(ldsb + lane * 16);
    fb = opaque(ldsb); fbw = opaque(ldsb + wid);
  }
  const unsigned epv = __hip_atomic_load((gu32*)(ws + WS_EP) + b * 32, RLX_AGENT);
  const unsigned own4 = (unsigned)wid * (4 * GS), oth4 = (unsigned)(wid ^ 2) * (4 * GS);

  bf16x8 fr[192], fx[32], qf[8], pf[8][2];
  f32x16 qacc[2] = {}, sacc[8], oacc[4] = {};
  float pv[8][16];
  unsigned pw[8][8], cntv[NST];
  float ts[8], mref = 0.f, alph[8], lsum = 0.f;
  unsigned long long slow = 0;
  constexpr float CS = 0.088388347648318440f * 1.4426950408889634f;
  constexpr float THI = 1099511627776.0f, TLO = 1.0f / 1099511627776.0f;

  auto issue_reads = [&](auto N) {
    constexpr int n = N, t = n / 8;
    if constexpr (n < 64) {
      constexpr int ks = qs_ks(n), i = qs_i(n);
      constexpr unsigned so = soff(cu(t)) + (4 * (ks & 1)) * GS;
      if constexpr (i == 0) fx[t * 4 + ks] = ldsr16(xa[(t & 1) * 4 + ks] + soff(xu(t)));
      fr[n] = trread2(wa[i][0][ks >> 1] + so, wa[i][1][ks >> 1] + so);
    } else if constexpr (n < 128) {
      fr[n] = ldsr16(ka[n % 8] + soff(cu(t)));
    } else {
      constexpr unsigned so = soff(cu(t)) + (4 * pv_ks(n)) * GS;
      fr[n] = trread2(va[pv_dt(n)][0] + so, va[pv_dt(n)][1] + so);
    }
  };
  auto wait_stage = [&](auto T) {
    constexpr int t = T;
    if (__builtin_expect((unsigned)__builtin_amdgcn_readfirstlane(cntv[t]) != ALL4, 0)) spin_flag(fb + F_CNT + 4 * t, ALL4);
    cfence();
  };
  auto qpack = [&](int i) {
    f32x16 t = qacc[i] * CS;
#pragma unroll
    for (int s = 0; s < 2; ++s) { qf[2 * i + s] = pack8(t, s); ldsw16(lin + own4 + (unsigned)(2 * i + s) * GS + FRG, qf[2 * i + s]); }
  };
  auto vop = [&](auto K) {
    constexpr VOp o = VS.op[(int)K];
    constexpr int j = o.tile, e = o.idx;
    if constexpr (o.type == OP_CHK) {
      alph[j] = 1.0f;
      const bool bad = j == 0 ? !(ts[0] >= TLO && ts[0] <= THI) : !(ts[j] <= THI);
      if (__builtin_expect((slow | __builtin_amdgcn_ballot_w64(bad)) != 0, 0)) {
        float m = sacc[j][0];
#pragma unroll
        for (int i = 1; i < 16; ++i) m = fmaxf(m, sacc[j][i]);
        m = swapmax(m);
        const float mn = j == 0 ? m : fmaxf(mref, m);
        if constexpr (j > 0) { alph[j] = __builtin_amdgcn_exp2f(mref - mn); lsum *= alph[j]; }
        mref = mn; slow = 1;
        f32x16 p; float sum = 0.f;
#pragma unroll
        for (int i = 0; i < 16; ++i) { p[i] = __builtin_amdgcn_exp2f(sacc[j][i] - mn); sum += p[i]; }
        ts[j] = sum; pf[j][0] = pack8(p, 0); pf[j][1] = pack8(p, 1);
      }
      lsum = pinf(lsum + ts[j]);
    } else if constexpr (o.type == OP_EX) {
      pv[j][e] = pinf(__builtin_amdgcn_exp2f(sacc[j][e]));
    } else if constexpr (o.type == OP_ADD) {
      if constexpr (e == 1) ts[j] = pinf(pv[j][0] + pv[j][1]); else ts[j] = pinf(ts[j] + pv[j][e]);
    } else {
      pw[j][e] = opaque(pk2(pv[j][2 * e], pv[j][2 * e + 1]));
      if constexpr ((e & 3) == 3) { u32x4 p = {pw[j][e - 3], pw[j][e - 2], pw[j][e - 1], pw[j][e]}; pf[j][e >> 2] = __builtin_bit_cast(bf16x8, p); }
    }
  };
  auto osend = [&](int i) {
#pragma unroll
    for (int s = 0; s < 2; ++s) ldsw16(lin + own4 + exc(i * 2 + s), pack8(oacc[2 + i], s));
  };
  float lw = 0.f;
  unsigned relq = 0;
#define CST(i)
  unsigned cst[32];

  spin_flag(fb + F_CNT, ALL4);
  STAMP(1);
  static_for<WLA>([&](auto N) { issue_reads(N); });
  SB();
  static_for<192>([&](auto N) {
    constexpr int n = N, t = n / 8;
    if constexpr (n >= 136 && n % 8 == 0) {
      if (__builtin_expect(slow != 0, 0)) {
#pragma unroll
        for (int dt = 0; dt < 4; ++dt) oacc[dt] *= alph[t - 16];
      }
    }
    if constexpr (n % 8 == 0) { if constexpr (n % 32 == 0) { CST(t); } else cst[t] = cst[t - 1]; }
    if constexpr (n < 64) {
      constexpr int ks = qs_ks(n), i = qs_i(n);
      qacc[i] = MFMA32(fr[n], fx[t * 4 + ks], qacc[i]);
    } else if constexpr (n < 128) {
      constexpr int j = t - 8, k = n % 8;
      if constexpr (k == 0) { f32x16 z = {}; sacc[j] = MFMA32(fr[n], qf[0], z); }
      else sacc[j] = MFMA32(fr[n], qf[k], sacc[j]);
    } else {
      oacc[pv_dt(n)] = MFMA32(fr[n], pf[t - 16][pv_ks(n)], oacc[pv_dt(n)]);
    }
    SB();
    if constexpr (n == 48) tag = next_tag((unsigned)__builtin_amdgcn_readfirstlane(epv));
    static_for<3>([&](auto I) {
      constexpr int m = first_read(n) + (int)I;
      if constexpr (m < 192 && n != 63) {
        if constexpr (isl(m) == n) {
          if constexpr (m % 8 == 0) wait_stage(std::integral_constant<int, m / 8>{});
          issue_reads(std::integral_constant<int, m>{});
          if constexpr (m % 8 == 7) { if constexpr (PL.rel[m / 8]) lds_set(fbw + F_REL + 4 * (m / 8)); }
        }
      }
    });
    static_for<NST>([&](auto T) { constexpr int tt = T; if constexpr (tt > 0 && tt != 8 && fpre(tt) == n) cntv[tt] = ldsflag(fb + F_CNT + 4 * tt); });
    if constexpr (n == 55 && PL.qrel >= 0) relq = ldsflag(fb + F_REL + 4 * PL.qrel);
    if constexpr (n == 61) {
      if constexpr (PL.qrel >= 0) { if (__builtin_expect((unsigned)__builtin_amdgcn_readfirstlane(relq) != ALL4, 0)) spin_flag(fb + F_REL + 4 * PL.qrel, ALL4); }
      cfence();
      qpack(0);
    }
    if constexpr (n == 184) relq = ldsflag(fb + F_REL + 4 * PL.erel);
    if constexpr (n == 63) {
      qpack(1);
      lds_set(fb + F_Q + 4 * wid);
      CST(26);
      spin_flag(fb + F_Q + 4 * (wid ^ 2), 1u);
      CST(27);
#pragma unroll
      for (int k = 0; k < 4; ++k) qf[4 + k] = ldsr16(lin + oth4 + (unsigned)k * GS + FRG);
      spin_flag(fb + F_CNT + 4 * 8, ALL4);
      static_for<WLA>([&](auto I) { issue_reads(std::integral_constant<int, 64 + (int)I>{}); });
    }
    if constexpr (n >= 64) static_for<VS.start[n + 1] - VS.start[n]>([&](auto K) { vop(std::integral_constant<int, VS.start[n] + (int)K>{}); });
    if constexpr (n == 188) {
      lw = swapadd(lsum);
      f32x2 st = {mref, lw};
      *(LAS f32x2*)(uintptr_t)(lin + (unsigned)(1 + wid) * GS + MISC) = st;
    }
    if constexpr (n == 189) {
      if (__builtin_expect((unsigned)__builtin_amdgcn_readfirstlane(relq) != ALL4, 0)) spin_flag(fb + F_REL + 4 * PL.erel, ALL4);
      cfence();
      osend(0);
    }
    if constexpr (n == 190) { osend(1); lds_set(fb + F_O1 + 4 * wid); }
    SB();
  });
  STAMP(30);
  CST(24);
  spin_flag(fb + F_O1 + 4 * (wid ^ 2), 1u);
  STAMP(31);
  const int lane = fresh_lane();
  const __amdgpu_buffer_rsrc_t wsr = __builtin_amdgcn_make_buffer_rsrc(ws, 0, 16u << 20, 0x00020000);
  bf16x8 of[4][4];
  {
    const f32x2 so2 = *(const LAS f32x2*)(uintptr_t)(lin + (unsigned)(1 + (wid ^ 2)) * GS + MISC);
    u32x4 v[4];
#pragma unroll
    for (int k = 0; k < 4; ++k) v[k] = *(const LAS u32x4*)(uintptr_t)(lin + oth4 + exc(k));
    const float M = fmaxf(mref, so2[0]);
    const float bs = __builtin_amdgcn_exp2f(mref - M), bo = __builtin_amdgcn_exp2f(so2[0] - M);
    const float inv = 1.0f / (lw * bs + so2[1] * bo);
    const float fs = bs * inv, fo2 = bo * inv;
    const unsigned goff = WS_O + (unsigned)(((bid * 2 + r) * 4 + 2 * c) * 2) * 1024 + lane * 16;
#pragma unroll
    for (int i = 0; i < 2; ++i) {
      f32x16 t;
#pragma unroll
      for (int e = 0; e < 16; ++e) {
        const unsigned w = v[i * 2 + (e >> 3)][(e & 7) >> 1];
        t[e] = fmaf(__uint_as_float((e & 1) ? (w & 0xffff0000u) : (w << 16)), fo2, oacc[i][e] * fs);
      }
#pragma unroll
      for (int s = 0; s < 2; ++s) {
        of[0][i * 2 + s] = pack8(t, s);
        __builtin_amdgcn_raw_buffer_store_b128(__builtin_bit_cast(u32x4, of[0][i * 2 + s]), wsr, goff + (i * 2 + s) * 1024, 0, 16);
      }
    }
    cfence();
  }
  unsigned ob[4][2], oa[2][4][2][2];
#pragma unroll
  for (int k = 0; k < 4; ++k)
#pragma unroll
    for (int tt = 0; tt < 2; ++tt) ob[k][tt] = ldsb + trV(lane, k ^ (2 * c), tt);
  auto oaddr = [&](auto JJ) {
    constexpr int jj = JJ;
    const unsigned so = c ? soff(28 + 2 * jj + 1) : soff(28 + 2 * jj);
#pragma unroll
    for (int k = 0; k < 4; ++k)
#pragma unroll
      for (int tt = 0; tt < 2; ++tt) { oa[jj & 1][k][tt][0] = opaque(ob[k][tt] + so); oa[jj & 1][k][tt][1] = opaque(ob[k][tt] + so + 8u * GS); }
  };
  bf16x8 fo[64];
  f32x16 pacc[4] = {};
  auto oreads = [&](auto M) {
    constexpr int m = M, ks4 = op_ks(m), k = op_ct(m), jj = m / 16;
    constexpr unsigned so = (4 * (ks4 & 1)) * GS;
    if constexpr (m % 16 == 0) { spin_flag(fb + F_WO + 4 * jj, ALL4); oaddr(std::integral_constant<int, jj>{}); }
    fo[m] = trread2(oa[jj & 1][k][0][ks4 >> 1] + so, oa[jj & 1][k][1][ks4 >> 1] + so);
    if constexpr (m == 15) lds_set(fbw + F_O2);
  };
  STAMP(32);
  asm volatile("s_waitcnt vmcnt(0)" ::: "memory");
  if (lane == 0) __hip_atomic_store((gu32*)(ws + WS_F1) + (b * 8 + g) * FSTR + hh * 4 + wid, tag, RLX_AGENT);
  STAMP(33);
  static_for<OLA0>([&](auto M) { oreads(M); });
  STAMP(34);
  {
    gu32* f1 = (gu32*)(ws + WS_F1) + (b * 8 + g) * FSTR + ((hh + 1 + min(lane, 2)) & 3) * 4 + wid;
    for (unsigned spins = 0; spins < (1u << 17); ++spins) {
      if (__all(__hip_atomic_load(f1, RLX_AGENT) == tag)) break;
    }
    asm volatile("" ::: "memory");
    STAMP(35);
#pragma unroll
    for (int j2 = 1; j2 < 4; ++j2) {
      const int sb = (bid & ~24) | (((hh + j2) & 3) << 3);
#pragma unroll
      for (int k = 0; k < 4; ++k)
        of[j2][k] = __builtin_bit_cast(bf16x8, __builtin_amdgcn_raw_buffer_load_b128(wsr, WS_O + (unsigned)((sb * 2 + r) * 8 + 4 * c + k) * 1024 + lane * 16, 0, 16));
    }
  }
  const unsigned xo = lin + (unsigned)r * (8 * GS) + (c ? soff(29) : soff(28)), xi = lin + (unsigned)r * (8 * GS) + (c ? soff(28) : soff(29));
  const unsigned xo0 = opaque(xo), xo1 = opaque(xo + 4 * GS), xi0 = opaque(xi), xi1 = opaque(xi + 4 * GS);
  unsigned relo = 0;
  SB();
  static_for<64>([&](auto M) {
    constexpr int m = M, jj = m / 16;
    pacc[op_ct(m)] = MFMA32(fo[m], of[jj][op_ks(m)], pacc[op_ct(m)]);
    SB();
    static_for<2>([&](auto I) {
      constexpr int mm = op_first(m) + (int)I;
      if constexpr (mm < 64) { if constexpr (op_isl(mm) == m) oreads(std::integral_constant<int, mm>{}); }
    });
    if constexpr (m == 50) relo = ldsflag(fb + F_O2);
    if constexpr (m == 57) {
      if (__builtin_expect((unsigned)__builtin_amdgcn_readfirstlane(relo) != ALL4, 0)) spin_flag(fb + F_O2, ALL4);
      cfence();
#pragma unroll
      for (int gq = 0; gq < 4; ++gq) { f32x4 v = {pacc[2][4 * gq], pacc[2][4 * gq + 1], pacc[2][4 * gq + 2], pacc[2][4 * gq + 3]}; *(LAS f32x4*)(uintptr_t)(xo0 + (unsigned)gq * GS) = v; }
    }
    if constexpr (m == 58) {
#pragma unroll
      for (int gq = 0; gq < 4; ++gq) { f32x4 v = {pacc[3][4 * gq], pacc[3][4 * gq + 1], pacc[3][4 * gq + 2], pacc[3][4 * gq + 3]}; *(LAS f32x4*)(uintptr_t)(xo1 + (unsigned)gq * GS) = v; }
      lds_set(fb + F_P + 4 * wid);
    }
    SB();
  });
  CST(25);
  STAMP(36);
  spin_flag(fb + F_P + 4 * (wid ^ 2), 1u);
  {
    f32x4 v[8];
#pragma unroll
    for (int k = 0; k < 8; ++k) v[k] = *(const LAS f32x4*)(uintptr_t)((k < 4 ? xi0 : xi1) + (unsigned)(k & 3) * GS);
#pragma unroll
    for (int ci = 0; ci < 2; ++ci) {
      const int cons = (bid & ~7) | (2 * (2 * c + ci) + r);
      f32x16 t;
#pragma unroll
      for (int e = 0; e < 16; ++e) t[e] = pacc[ci][e] + v[ci * 4 + (e >> 2)][e & 3];
#pragma unroll
      for (int s = 0; s < 2; ++s)
        __builtin_amdgcn_raw_buffer_store_b128(__builtin_bit_cast(u32x4, pack8(t, s)), wsr, WS_P + (unsigned)((cons * 8 + g) * 2 + s) * 1024 + lane * 16, 0, 16);
    }
  }
  asm volatile("s_waitcnt vmcnt(0)" ::: "memory");
  if (lane < 2) {
    const int cons = (bid & ~7) | (2 * (2 * c + lane) + r);
    __hip_atomic_store((gu32*)(ws + WS_F2) + cons * FSTR + g, tag, RLX_AGENT);
  }
  STAMP(37);
  {
    gu32* f2 = (gu32*)(ws + WS_F2) + bid * FSTR + (lane & 7);
    const unsigned poff = WS_P + (unsigned)((bid * 8 + (lane >> 5) * 4) * 2 + (wid & 1)) * 1024 + ((wid >> 1) * 32 + (lane & 31)) * 16;
    for (unsigned spins = 0; spins < (1u << 17); ++spins) {
      if (__all(__hip_atomic_load(f2, RLX_AGENT) == tag)) break;
    }
    asm volatile("" ::: "memory");
    STAMP(38);
    if ((bid & 31) == 0 && wid == 0 && lane == 0) __hip_atomic_store((gu32*)(ws + WS_EP) + b * 32, tag, RLX_AGENT);
    u32x4 pp[4];
#pragma unroll
    for (int p = 0; p < 4; ++p) pp[p] = __builtin_amdgcn_raw_buffer_load_b128(wsr, poff + p * 2048, 0, 16);
    float sm[8];
#pragma unroll
    for (int e = 0; e < 8; ++e) {
      float q4[4];
#pragma unroll
      for (int p = 0; p < 4; ++p) { const unsigned w = pp[p][e >> 1]; q4[p] = __uint_as_float((e & 1) ? (w & 0xffff0000u) : (w << 16)); }
      sm[e] = (q4[0] + q4[1]) + (q4[2] + q4[3]);
    }
    float s[4];
#pragma unroll
    for (int e = 0; e < 4; ++e) {
      auto rr = __builtin_amdgcn_permlane32_swap(__float_as_uint(sm[e]), __float_as_uint(sm[4 + e]), false, false);
      s[e] = __uint_as_float(rr[0]) + __uint_as_float(rr[1]);
    }
    const int ct = g >> 1, rr = g & 1;
    u32x2 o2 = {pk2(s[0], s[1]), pk2(s[2], s[3])};
    *(u32x2*)(out + (size_t)(b * 64 + rr * 32 + (lane & 31)) * 512 + hh * 128 + ct * 32 + 8 * (2 * (wid & 1) + (lane >> 5)) + 4 * (wid >> 1)) = o2;
  }
  STAMP(39);
}

extern "C" void kernel_launch(void* const* d_in, const int* in_sizes, int n_in, void* d_out, int out_size, void* d_ws, size_t ws_size, hipStream_t stream) {
  (void)hipFuncSetAttribute((const void*)gqa_kernel, hipFuncAttributeMaxDynamicSharedMemorySize, LDSB);
  gqa_kernel<<<256, 512, LDSB, stream>>>((const u16*)d_in[0], (const u16*)d_in[1], (const u16*)d_in[2], (const u16*)d_in[3], (const u16*)d_in[4], (char*)d_ws, (u16*)d_out);
}
```

```cpp
#include <hip/hip_runtime.h>
#include <hip/hip_bf16.h>
#include <utility>

typedef unsigned short u16;
typedef __bf16 bf16x8 __attribute__((ext_vector_type(8)));
typedef __bf16 bf16x2 __attribute__((ext_vector_type(2)));
typedef short s16x4 __attribute__((ext_vector_type(4)));
typedef float f32x16 __attribute__((ext_vector_type(16)));
typedef float f32x4 __attribute__((ext_vector_type(4)));
typedef float f32x2 __attribute__((ext_vector_type(2)));
typedef unsigned u32x4 __attribute__((ext_vector_type(4)));
typedef unsigned u32x2 __attribute__((ext_vector_type(2)));
#define LAS __attribute__((address_space(3)))
#define GAS __attribute__((address_space(1)))
typedef GAS unsigned gu32;
#define DI __device__ __forceinline__
#define MFMA32(a, b, c) __builtin_amdgcn_mfma_f32_32x32x16_bf16((a), (b), (c), 0, 0, 0)
#define RLX_AGENT __ATOMIC_RELAXED, __HIP_MEMORY_SCOPE_AGENT
#define SB() __builtin_amdgcn_sched_barrier(0)

DI unsigned pk2(float a, float b) { f32x2 v = {a, b}; bf16x2 r = __builtin_convertvector(v, bf16x2); return __builtin_bit_cast(unsigned, r); }
DI bf16x8 pack8(const f32x16& x, int s) {
  u32x4 p = {pk2(x[8 * s], x[8 * s + 1]), pk2(x[8 * s + 2], x[8 * s + 3]), pk2(x[8 * s + 4], x[8 * s + 5]), pk2(x[8 * s + 6], x[8 * s + 7])};
  return __builtin_bit_cast(bf16x8, p);
}
DI bf16x8 trread2(unsigned a0, unsigned a1) {
  s16x4 lo = __builtin_amdgcn_ds_read_tr16_b64_v4i16((LAS s16x4*)(uintptr_t)a0);
  s16x4 hi = __builtin_amdgcn_ds_read_tr16_b64_v4i16((LAS s16x4*)(uintptr_t)a1);
  return __builtin_bit_cast(bf16x8, __builtin_shufflevector(lo, hi, 0, 1, 2, 3, 4, 5, 6, 7));
}
DI bf16x8 ldsr16(unsigned a) { return *(const LAS bf16x8*)(uintptr_t)a; }
DI void ldsw16(unsigned a, bf16x8 v) { *(LAS bf16x8*)(uintptr_t)a = v; }
template <bool NT = false> DI void glds16(const void* sbase, unsigned voff, unsigned lds_dst) {
  unsigned keep;
  if constexpr (NT)
    asm volatile("s_mov_b32 %0, m0\n\ts_mov_b32 m0, %3\n\ts_nop 0\n\tglobal_load_lds_dwordx4 %1, %2 nt\n\ts_mov_b32 m0, %0"
                 : "=&s"(keep) : "v"(voff), "s"(sbase), "s"(lds_dst) : "memory");
  else
    asm volatile("s_mov_b32 %0, m0\n\ts_mov_b32 m0, %3\n\ts_nop 0\n\tglobal_load_lds_dwordx4 %1, %2\n\ts_mov_b32 m0, %0"
                 : "=&s"(keep) : "v"(voff), "s"(sbase), "s"(lds_dst) : "memory");
}
#define RMASK 0xfffffffffull
DI void gld16(u32x4& r, const void* sbase, unsigned voff) { asm volatile("global_load_dwordx4 %0, %1, %2" : "=v"(r) : "v"(voff), "s"(sbase) : "memory"); }
#define NTMASK 0ull
template <int N> DI void wait_vm() { asm volatile("s_waitcnt vmcnt(%0)" ::"n"(N) : "memory"); }
template <int N> DI void wait_lgkm() { asm volatile("s_waitcnt lgkmcnt(%0)" ::"n"(N) : "memory"); }
DI void rawbar() { asm volatile("" ::: "memory"); __builtin_amdgcn_s_barrier(); asm volatile("" ::: "memory"); }
DI float swapmax(float m) { auto rr = __builtin_amdgcn_permlane32_swap(__float_as_uint(m), __float_as_uint(m), false, false); return fmaxf(__uint_as_float(rr[0]), __uint_as_float(rr[1])); }
DI float swapadd(float m) { auto rr = __builtin_amdgcn_permlane32_swap(__float_as_uint(m), __float_as_uint(m), false, false); return __uint_as_float(rr[0]) + __uint_as_float(rr[1]); }
DI unsigned opaque(unsigned v) { asm volatile("" : "+v"(v)); return v; }
DI float pinf(float v) { asm volatile("" : "+v"(v)); return v; }
DI float max3(float a, float b, float c) { float r; asm("v_max3_f32 %0, %1, %2, %3" : "=v"(r) : "v"(a), "v"(b), "v"(c)); return r; }
DI int fresh_lane() { int l; asm volatile("v_mbcnt_lo_u32_b32 %0, -1, 0\n\tv_mbcnt_hi_u32_b32 %0, -1, %0" : "=v"(l)); return l; }

template <typename F, int... I> DI void static_for_impl(F&& f, std::integer_sequence<int, I...>) { (f(std::integral_constant<int, I>{}), ...); }
template <int N, typename F> DI void static_for(F&& f) { static_for_impl(f, std::make_integer_sequence<int, N>{}); }

constexpr int NSLOT = 10, GS = NSLOT * 1024, LDSB = 16 * GS, NUNITS = 36, NST = 24;
DI int swz(int R) { return ((R & 3) << 2) | ((R >> 2) & 3); }
DI unsigned rowbase(int R, int hf) { return (unsigned)((R >> 2) * GS + (R & 3) * 256 + 16 * (hf ^ swz(R))); }
DI unsigned trW(int lane, int dt, int t) {
  const int hf = lane >> 5, q4 = (lane & 15) >> 2, p4 = lane & 3, blk = (lane >> 4) & 1;
  const int pc = ((p4 & 1) << 1) | (p4 >> 1);
  return (unsigned)((2 * hf + t) * GS + q4 * 256 + 16 * (((dt ^ q4) << 2) | ((2 * blk + (pc >> 1)) ^ (2 * hf + t))) + 8 * (pc & 1));
}
DI unsigned trV(int lane, int dt, int t) {
  const int hf = lane >> 5, q4 = (lane & 15) >> 2, p4 = lane & 3, blk = (lane >> 4) & 1;
  return (unsigned)((2 * t + hf) * GS + q4 * 256 + 16 * (((dt ^ q4) << 2) | ((2 * blk + (p4 >> 1)) ^ (2 * t + hf))) + 8 * (p4 & 1));
}

#define WLA 4
#define LDEPTH 4
#define TK -1
#define TV -1
#define TW 27
constexpr int cu(int t) { return t < 8 ? (t / 2) * 3 + 1 + (t & 1) : t + 4; }
constexpr int xu(int t) { return (t / 2) * 3; }
constexpr int stage_of(int u) { for (int t = 0; t < NST; ++t) if (cu(t) == u) return t; return -1; }
#define ORD 0, 1, 2, 3, 4, 5, 6, 7, 8, 9, 10, 11, 12, 13, 14, 15, 16, 17, 18, 19, 20, 21, 22, 23, 24, 25, 26, 27, 28, 29, 30, 31, 32, 33, 34, 35
#define EPOS 34
#define RELMASK 0x1ffffff
#define QPOS 15
struct Plan { int order[NUNITS], slot[NUNITS], need[NUNITS], qslot, qrel, e0, e1, erel; bool rel[26]; };
constexpr int last_use(int u) { return u < 12 ? (u % 3 == 0 ? (u / 3) * 2 + 1 : stage_of(u)) : (u < 28 ? u - 4 : 99); }
constexpr Plan make_plan() {
  Plan P{};
  { const int o[NUNITS] = {ORD}; for (int i = 0; i < NUNITS; ++i) P.order[i] = o[i]; }
  int fr[9] = {-1, -1, -1, -1, -1, -1, -1, -1, -1};
  auto take = [&](int until) { int bs = 0; for (int k = 1; k < 9; ++k) if (fr[k] < fr[bs]) bs = k; int f = fr[bs]; fr[bs] = until; return bs * 100 + (f + 1); };
  int run = -1;
  for (int p = 0; p < NUNITS; ++p) {
    if (p == QPOS) { int t = take(8); P.qslot = t / 100; P.qrel = t % 100 - 1; }
    if (p == EPOS) { int t = take(99); P.e0 = t / 100; P.erel = t % 100 - 1; }
    const int u = P.order[p];
    int t = take(last_use(u));
    P.slot[u] = t / 100;
    int f = t % 100 - 1;
    while (f >= 0 && !((RELMASK >> f) & 1)) ++f;
    if (f > run) { run = f; P.need[p] = f; P.rel[f] = true; } else P.need[p] = -1;
  }
  if (P.qrel >= 0) P.rel[P.qrel] = true;
  P.rel[P.erel] = true;
  return P;
}
constexpr Plan PL = make_plan();
constexpr bool no_early_flag() { for (int p = 0; p <= LDEPTH + 1; ++p) if (PL.need[p] >= 0) return false; return true; }
static_assert(PL.order[0] == 0 && no_early_flag() && LDEPTH <= 12, "no flag access before the barrier");
constexpr bool rmp(int p) { return ((RMASK) >> PL.order[p]) & 1; }
constexpr bool dma_first() { for (int p = 0; p + 1 < NUNITS; ++p) if (rmp(p) && !rmp(p + 1)) return false; return true; }
static_assert(dma_first(), "releases are checked in stream order");
static_assert(PL.qrel <= 6 && PL.erel <= 22, "plan");
constexpr int first_use(int u) { return u < 12 ? (u % 3 == 0 ? (u / 3) * 2 : stage_of(u)) : (u < 28 ? u - 4 : 24); }
constexpr bool plan_ok() {
  for (int p = 0; p < NUNITS; ++p) {
    for (int pp = p; pp < NUNITS; ++pp) if (PL.need[p] >= 0 && first_use(PL.order[pp]) <= PL.need[p]) return false;
    for (int pp = p + 1; pp < NUNITS; ++pp) {
      const int u = PL.order[p], v = PL.order[pp];
      if (u == v || (v < 12 && v % 3 == 0 && u < 12 && u / 3 == v / 3) || (v >= 28 && u == v + 1 && !(v & 1))) return false;
    }
  }
  return true;
}
static_assert(plan_ok(), "plan");
constexpr int ntouch(int p) { return (p == TK ? 2 : 0) + (p == TV ? 2 : 0) + (p == TW ? 1 : 0); }
constexpr int ntouch_in(int q, int last) { int n = 0; for (int p = q; p <= last; ++p) n += ntouch(p); return n; }
constexpr unsigned soff(int u) { return (unsigned)PL.slot[u] * 1024u; }
constexpr unsigned exc(int k) { return (unsigned)(k * GS + PL.e0 * 1024); }
constexpr unsigned FRG = PL.qslot * 1024, MISC = 9216;
constexpr unsigned F_CNT = MISC, F_WO = MISC + 96, F_Q = MISC + 112, F_O1 = MISC + 128, F_O2 = MISC + 144, F_REL = MISC + 160, F_P = MISC + 288;

#define FSTR 32
constexpr unsigned WS_O = 0, WS_P = 4u << 20, WS_F1 = 12u << 20, WS_F2 = WS_F1 + 64 * FSTR * 4 + 65536, WS_EP = WS_F2 + 256 * FSTR * 4 + 65536;
static_assert(WS_EP + 1024 <= (16u << 20), "workspace layout");

DI unsigned next_tag(unsigned e) { return e * 0x9E3779B1u + 0x7F4A7C15u; }
#define LOADER_FIRST(x) __builtin_expect((x), 1)
DI unsigned ldsflag(unsigned a) { return *(volatile const LAS unsigned*)(uintptr_t)a; }
DI void cfence() { asm volatile("" ::: "memory"); }
DI void spin_flag(unsigned a, unsigned want) {
  for (unsigned s = 0; s < (1u << 22); ++s) { if ((unsigned)__builtin_amdgcn_readfirstlane(ldsflag(a)) == want) break; }
  cfence();
}
#define LSLEEP 1
DI void spin_flag_slow(unsigned a, unsigned want) {
  for (unsigned s = 0; s < (1u << 20); ++s) { if ((unsigned)__builtin_amdgcn_readfirstlane(ldsflag(a)) == want) break; if (LSLEEP) __builtin_amdgcn_s_sleep(LSLEEP); }
  cfence();
}
constexpr unsigned ALL4 = 0x01010101u;
DI void lds_set(unsigned a) { cfence(); *(volatile LAS unsigned char*)(uintptr_t)a = 1; cfence(); }

#define WLA2 8
constexpr int isl(int m) { return m < 64 ? m - WLA : (m < 64 + WLA ? 63 : (64 + (m - 64 - WLA) / 2 > m - WLA2 ? 64 + (m - 64 - WLA) / 2 : m - WLA2)); }
#define FPRE 5
constexpr int fpre(int t) { int n = isl(8 * t) - FPRE; if (t > 8 && n < 64) n = 64; return n < 0 ? 0 : n; }
constexpr int first_read(int n) { for (int m = 0; m < 192; ++m) if (isl(m) >= n) return m; return 192; }
constexpr int qs_ks(int n) { return n < 56 ? (n % 8) / 2 : (n % 4); }
constexpr int qs_i(int n) { return n < 56 ? n % 2 : (n % 8) / 4; }
constexpr int pv_ks(int n) { return n < 184 ? (n % 8) / 4 : (n & 3) >> 1; }
constexpr int pv_dt(int n) { return n < 184 ? n % 4 : (n < 188 ? 2 : 0) + (n & 1); }

constexpr int op_ks(int m) { return m < 48 ? (m % 16) / 4 : (m % 8) / 2; }
constexpr int op_ct(int m) { return m < 48 ? m % 4 : (m < 56 ? 2 : 0) + (m & 1); }
#define OLA0 8
#define OLA 12
constexpr int op_isl(int m) { return m < OLA0 ? -1 : ((m - OLA0) / 2 > m - OLA ? (m - OLA0) / 2 : m - OLA); }
constexpr int op_first(int n) { for (int m = OLA0; m < 64; ++m) if (op_isl(m) >= n) return m; return 64; }
enum { OP_CHK, OP_EX, OP_ADD, OP_CVT };
struct VOp { int type, tile, idx; };
struct VSched { VOp op[512]; int start[194]; };
#define BQK 25
#define BPV 20
constexpr VSched make_sched() {
  VSched s{};
  VOp ex[48] = {};
  int k = 0;
  for (int e = 0; e < 16; ++e) {
    ex[k++] = VOp{OP_EX, 0, e};
    if (e >= 2) ex[k++] = VOp{OP_ADD, 0, e - 1};
    if (e >= 2 && (e & 1) == 0) ex[k++] = VOp{OP_CVT, 0, (e - 2) / 2};
  }
  ex[k++] = VOp{OP_ADD, 0, 15};
  ex[k++] = VOp{OP_CVT, 0, 7};
  ex[k++] = VOp{OP_CHK, 0, 0};
  const int len = k;
  int je = 0, ke = 0, no = 0, carry = 0;
  for (int n = 0; n < 192; ++n) {
    s.start[n] = no;
    if (n < 64) continue;
    int bud = (n < 128 ? BQK : BPV) + carry;
    while (je < 8 && n >= 64 + 8 * je + 7 + 2) {
      VOp o = ex[ke]; o.tile = je;
      int cost = o.type == OP_EX ? 11 : (o.type == OP_CHK ? 14 : 5);
      int dl = 128 + 8 * je - 2 - (len - 1 - ke) / 4;
      if (bud < cost && n < dl) break;
      s.op[no++] = o; bud -= cost;
      if (++ke == len) { ++je; ke = 0; }
    }
    carry = bud > 6 ? 6 : (bud < -6 ? -6 : bud);
  }
  s.start[192] = no; s.start[193] = je == 8 ? 1 : 0;
  return s;
}
constexpr VSched VS = make_sched();
static_assert(VS.start[193] == 1, "schedule incomplete");

#define STAMP(i)

__global__ __launch_bounds__(512, 2) void gqa_kernel(const u16* __restrict__ x, const u16* __restrict__ kc, const u16* __restrict__ vc,
                                                     const u16* __restrict__ wq, const u16* __restrict__ wo, char* __restrict__ ws, u16* __restrict__ out) {
  extern __shared__ __attribute__((aligned(16))) char lds[];
  const int wid8 = __builtin_amdgcn_readfirstlane(threadIdx.x >> 6);
  const int wid = wid8 & 3;
  const int bid = blockIdx.x;
  const int g = bid & 7, b = bid >> 5, hh = (bid >> 3) & 3, h = g * 4 + hh;
  const unsigned ldsb = (unsigned)(uintptr_t)(LAS char*)lds;

  if (LOADER_FIRST(wid8 >= 4)) {
    const int lane = fresh_lane();
    unsigned voffx[4], voffw[4], voffk[4];
#pragma unroll
    for (int i = 0; i < 4; ++i) {
      unsigned row = wid * 16 + (lane >> 4) + 4 * i;
      unsigned ch = (lane & 15) ^ (((lane >> 4) << 2) | i);
      voffx[i] = row * 1024 + ch * 16; voffw[i] = row * 8192 + ch * 16; voffk[i] = row * 256 + ch * 16;
    }
    u32x4 rg[LDEPTH][4];
    const unsigned lq = opaque(ldsb + wid * (4 * GS) + lane * 16);
    auto issue = [&](auto P) {
      constexpr int p = P, u = PL.order[p];
      constexpr bool nt = ((NTMASK) >> u) & 1, rm = ((RMASK) >> u) & 1;
      const unsigned dst = ldsb + soff(u) + wid * (4 * GS);
      auto go = [&](const u16* src, const unsigned (&vo)[4]) {
#pragma unroll
        for (int i = 0; i < 4; ++i) { if constexpr (rm) gld16(rg[p % LDEPTH][i], src, vo[i]); else glds16<nt>(src, vo[i], dst + i * GS); }
      };
      if constexpr (u < 12) {
        const int q = ((u / 3) + (b >> 1) + hh) & 3; constexpr int rr = u % 3;
        if constexpr (rr == 0) go(x + (size_t)b * 64 * 512 + q * 128, voffx);
        else { int wu = q * 2 + ((rr - 1) ^ (b & 1)); go(wq + (size_t)wu * 64 * 4096 + h * 128, voffw); }
      } else if constexpr (u < 28) {
        int k = (u - 12 + 2 * hh) & 7; go((u < 20 ? kc : vc) + ((size_t)(b * 8 + g) * 512 + k * 64) * 128, voffk);
      } else {
        int k = (u - 28 + 2 * hh) & 7; go(wo + ((size_t)g * 512 + k * 64) * 512 + hh * 128, voffx);
      }
    };
#define LST(i)
    const unsigned fb = opaque(ldsb), fbw = opaque(ldsb + wid);
    unsigned relw = 0;
    auto signal = [&](auto Q) {
      constexpr int u = PL.order[(int)Q];
      if constexpr (((RMASK) >> u) & 1) {
        constexpr int k = PL.need[(int)Q];
        if constexpr (k >= 0) {
          if ((unsigned)__builtin_amdgcn_readfirstlane(relw) != ALL4) spin_flag_slow(fb + F_REL + 4 * k, ALL4);
          cfence();
        }
        u32x4 (&r)[4] = rg[(int)Q % LDEPTH];
        asm volatile("" : "+v"(r[0]), "+v"(r[1]), "+v"(r[2]), "+v"(r[3]) :: "memory");
#pragma unroll
        for (int i = 0; i < 4; ++i) *(LAS u32x4*)(uintptr_t)(lq + soff(u) + i * GS) = r[i];
        cfence();
      }
      if constexpr (u < 28) { if constexpr (stage_of(u) >= 0) { lds_set(fbw + F_CNT + 4 * stage_of(u)); LST(stage_of(u)); } }
      else if constexpr (u & 1) { lds_set(fbw + F_WO + 4 * ((u - 28) >> 1)); LST(25 + ((u - 28) >> 1)); }
    };
    unsigned tch[5] = {0, 0, 0, 0, 0};
    auto post = [&](auto P) {
      constexpr int p = P;
      issue(P);
      if constexpr (p == TK) {
        const char* tp = (const char*)(kc + ((size_t)(b * 8 + g) * 512 + hh * 128) * 128) + (wid * 64 + lane) * 128;
        asm volatile("global_load_dword %0, %1, off" : "=v"(tch[0]) : "v"(tp) : "memory");
        asm volatile("global_load_dword %0, %1, off offset:64" : "=v"(tch[3]) : "v"(tp) : "memory");
      }
      if constexpr (p == TV) {
        const char* tp = (const char*)(vc + ((size_t)(b * 8 + g) * 512 + hh * 128) * 128) + (wid * 64 + lane) * 128;
        asm volatile("global_load_dword %0, %1, off" : "=v"(tch[1]) : "v"(tp) : "memory");
        asm volatile("global_load_dword %0, %1, off offset:64" : "=v"(tch[4]) : "v"(tp) : "memory");
      }
      if constexpr (p == TW) {
        const char* tp = (const char*)(wo + (size_t)g * 512 * 512) + (bid >> 3) * 16384 + (wid * 64 + lane) * 64;
        asm volatile("global_load_dword %0, %1, off" : "=v"(tch[2]) : "v"(tp) : "memory");
      }
    };
    LST(31);
    static_for<LDEPTH - 1>([&](auto P) { post(P); });
    int sdone = -1;
    unsigned relv = 0;
    static_for<NUNITS>([&](auto Q) {
      constexpr int q = Q, p = q + LDEPTH - 1;
      if constexpr (p < NUNITS) {
        constexpr int k = PL.need[p];
        if constexpr (k >= 0 && !rmp(p)) {
          if ((unsigned)__builtin_amdgcn_readfirstlane(relv) != ALL4) {
            static_for<LDEPTH - 1>([&](auto I) {
              constexpr int qq = q + (int)I;
              wait_vm<(p - 1 - qq) * 4 + ntouch_in(qq, p - 1)>();
              if (qq > sdone) signal(std::integral_constant<int, qq>{});
            });
            sdone = p - 1;
            spin_flag_slow(fb + F_REL + 4 * k, ALL4);
          }
          cfence();
        }
        if constexpr (p + 1 < NUNITS) { constexpr int k1 = PL.need[p + 1]; if constexpr (k1 >= 0 && !rmp(p + 1)) relv = ldsflag(fb + F_REL + 4 * k1); }
        post(std::integral_constant<int, p>{});
      }
      constexpr int last = p < NUNITS ? p : NUNITS - 1;
      if constexpr (rmp(q) && PL.need[q] >= 0) relw = ldsflag(fb + F_REL + 4 * PL.need[q]);
      wait_vm<(last - q) * 4 + ntouch_in(q, last)>();
      if constexpr (q == 1) rawbar();
      if (q > sdone) signal(Q);
    });
    wait_vm<0>();
    asm volatile("" :: "v"(tch[0]), "v"(tch[1]), "v"(tch[2]), "v"(tch[3]), "v"(tch[4]));
    return;
  }

  __builtin_amdgcn_s_setprio(2);
  const int r = wid & 1, c = wid >> 1;
  STAMP(0);
  unsigned tag = 0;
  unsigned xa[8], wa[2][2][2], ka[8], va[4][2], lin = 0, fb = 0, fbw = 0;
  {
    const int lane = fresh_lane();
    if (wid == 0) { u32x2 z = {0u, 0u}; *(LAS u32x2*)(uintptr_t)(ldsb + MISC + lane * 8) = z; }
    wait_lgkm<0>();
    rawbar();
    const unsigned xb = (ldsb + rowbase(32 * r + (lane & 31), lane >> 5)) ^ (unsigned)((b & 1) * 128);
#pragma unroll
    for (int j = 0; j < 8; ++j) xa[j] = opaque(xb ^ (unsigned)(32 * j));
#pragma unroll
    for (int i = 0; i < 2; ++i)
#pragma unroll
      for (int t = 0; t < 2; ++t) { wa[i][t][0] = opaque(ldsb + trW(lane, 2 * c + i, t)); wa[i][t][1] = opaque(ldsb + trW(lane, 2 * c + i, t) + 8u * GS); }
    const unsigned kb = ldsb + rowbase(32 * c + (lane & 31), lane >> 5);
#pragma unroll
    for (int j = 0; j < 8; ++j) ka[j] = opaque(kb ^ (unsigned)(32 * (j ^ (4 * c))));
#pragma unroll
    for (int dt = 0; dt < 4; ++dt)
#pragma unroll
      for (int tt = 0; tt < 2; ++tt) va[dt][tt] = opaque(ldsb + trV(lane, dt ^ (2 * c), tt) + (unsigned)c * (8u * GS));
    lin = opaque(ldsb + lane * 16);
    fb = opaque(ldsb); fbw = opaque(ldsb + wid);
  }
  const unsigned epv = __hip_atomic_load((gu32*)(ws + WS_EP) + b * 32, RLX_AGENT);
  const unsigned own4 = (unsigned)wid * (4 * GS), oth4 = (unsigned)(wid ^ 2) * (4 * GS);

  bf16x8 fr[192], fx[32], qf[8], pf[8][2];
  f32x16 qacc[2] = {}, sacc[8], oacc[4] = {};
  float pv[8][16];
  unsigned pw[8][8], cntv[NST];
  float ts[8], mref = 0.f, alph[8], lsum = 0.f;
  unsigned long long slow = 0;
  constexpr float CS = 0.088388347648318440f * 1.4426950408889634f;
  constexpr float THI = 1099511627776.0f, TLO = 1.0f / 1099511627776.0f;

  auto issue_reads = [&](auto N) {
    constexpr int n = N, t = n / 8;
    if constexpr (n < 64) {
      constexpr int ks = qs_ks(n), i = qs_i(n);
      constexpr unsigned so = soff(cu(t)) + (4 * (ks & 1)) * GS;
      if constexpr (i == 0) fx[t * 4 + ks] = ldsr16(xa[(t & 1) * 4 + ks] + soff(xu(t)));
      fr[n] = trread2(wa[i][0][ks >> 1] + so, wa[i][1][ks >> 1] + so);
    } else if constexpr (n < 128) {
      fr[n] = ldsr16(ka[n % 8] + soff(cu(t)));
    } else {
      constexpr unsigned so = soff(cu(t)) + (4 * pv_ks(n)) * GS;
      fr[n] = trread2(va[pv_dt(n)][0] + so, va[pv_dt(n)][1] + so);
    }
  };
  auto wait_stage = [&](auto T) {
    constexpr int t = T;
    if (__builtin_expect((unsigned)__builtin_amdgcn_readfirstlane(cntv[t]) != ALL4, 0)) spin_flag(fb + F_CNT + 4 * t, ALL4);
    cfence();
  };
  auto qpack = [&](int i) {
    f32x16 t = qacc[i] * CS;
#pragma unroll
    for (int s = 0; s < 2; ++s) { qf[2 * i + s] = pack8(t, s); ldsw16(lin + own4 + (unsigned)(2 * i + s) * GS + FRG, qf[2 * i + s]); }
  };
  auto vop = [&](auto K) {
    constexpr VOp o = VS.op[(int)K];
    constexpr int j = o.tile, e = o.idx;
    if constexpr (o.type == OP_CHK) {
      alph[j] = 1.0f;
      const bool bad = j == 0 ? !(ts[0] >= TLO && ts[0] <= THI) : !(ts[j] <= THI);
      if (__builtin_expect((slow | __builtin_amdgcn_ballot_w64(bad)) != 0, 0)) {
        float m = sacc[j][0];
#pragma unroll
        for (int i = 1; i < 16; ++i) m = fmaxf(m, sacc[j][i]);
        m = swapmax(m);
        const float mn = j == 0 ? m : fmaxf(mref, m);
        if constexpr (j > 0) { alph[j] = __builtin_amdgcn_exp2f(mref - mn); lsum *= alph[j]; }
        mref = mn; slow = 1;
        f32x16 p; float sum = 0.f;
#pragma unroll
        for (int i = 0; i < 16; ++i) { p[i] = __builtin_amdgcn_exp2f(sacc[j][i] - mn); sum += p[i]; }
        ts[j] = sum; pf[j][0] = pack8(p, 0); pf[j][1] = pack8(p, 1);
      }
      lsum = pinf(lsum + ts[j]);
    } else if constexpr (o.type == OP_EX) {
      pv[j][e] = pinf(__builtin_amdgcn_exp2f(sacc[j][e]));
    } else if constexpr (o.type == OP_ADD) {
      if constexpr (e == 1) ts[j] = pinf(pv[j][0] + pv[j][1]); else ts[j] = pinf(ts[j] + pv[j][e]);
    } else {
      pw[j][e] = opaque(pk2(pv[j][2 * e], pv[j][2 * e + 1]));
      if constexpr ((e & 3) == 3) { u32x4 p = {pw[j][e - 3], pw[j][e - 2], pw[j][e - 1], pw[j][e]}; pf[j][e >> 2] = __builtin_bit_cast(bf16x8, p); }
    }
  };
  auto osend = [&](int i) {
#pragma unroll
    for (int s = 0; s < 2; ++s) ldsw16(lin + own4 + exc(i * 2 + s), pack8(oacc[2 + i], s));
  };
  float lw = 0.f;
  unsigned relq = 0;
#define CST(i)
  unsigned cst[32];

  spin_flag(fb + F_CNT, ALL4);
  STAMP(1);
  static_for<WLA>([&](auto N) { issue_reads(N); });
  SB();
  static_for<192>([&](auto N) {
    constexpr int n = N, t = n / 8;
    if constexpr (n >= 136 && n % 8 == 0) {
      if (__builtin_expect(slow != 0, 0)) {
#pragma unroll
        for (int dt = 0; dt < 4; ++dt) oacc[dt] *= alph[t - 16];
      }
    }
    if constexpr (n % 8 == 0) { if constexpr (n % 32 == 0) { CST(t); } else cst[t] = cst[t - 1]; }
    if constexpr (n < 64) {
      constexpr int ks = qs_ks(n), i = qs_i(n);
      qacc[i] = MFMA32(fr[n], fx[t * 4 + ks], qacc[i]);
    } else if constexpr (n < 128) {
      constexpr int j = t - 8, k = n % 8;
      if constexpr (k == 0) { f32x16 z = {}; sacc[j] = MFMA32(fr[n], qf[0], z); }
      else sacc[j] = MFMA32(fr[n], qf[k], sacc[j]);
    } else {
      oacc[pv_dt(n)] = MFMA32(fr[n], pf[t - 16][pv_ks(n)], oacc[pv_dt(n)]);
    }
    SB();
    if constexpr (n == 48) tag = next_tag((unsigned)__builtin_amdgcn_readfirstlane(epv));
    static_for<3>([&](auto I) {
      constexpr int m = first_read(n) + (int)I;
      if constexpr (m < 192 && n != 63) {
        if constexpr (isl(m) == n) {
          if constexpr (m % 8 == 0) wait_stage(std::integral_constant<int, m / 8>{});
          issue_reads(std::integral_constant<int, m>{});
          if constexpr (m % 8 == 7) { if constexpr (PL.rel[m / 8]) lds_set(fbw + F_REL + 4 * (m / 8)); }
        }
      }
    });
    static_for<NST>([&](auto T) { constexpr int tt = T; if constexpr (tt > 0 && tt != 8 && fpre(tt) == n) cntv[tt] = ldsflag(fb + F_CNT + 4 * tt); });
    if constexpr (n == 55 && PL.qrel >= 0) relq = ldsflag(fb + F_REL + 4 * PL.qrel);
    if constexpr (n == 61) {
      if constexpr (PL.qrel >= 0) { if (__builtin_expect((unsigned)__builtin_amdgcn_readfirstlane(relq) != ALL4, 0)) spin_flag(fb + F_REL + 4 * PL.qrel, ALL4); }
      cfence();
      qpack(0);
    }
    if constexpr (n == 184) relq = ldsflag(fb + F_REL + 4 * PL.erel);
    if constexpr (n == 63) {
      qpack(1);
      lds_set(fb + F_Q + 4 * wid);
      CST(26);
      spin_flag(fb + F_Q + 4 * (wid ^ 2), 1u);
      CST(27);
#pragma unroll
      for (int k = 0; k < 4; ++k) qf[4 + k] = ldsr16(lin + oth4 + (unsigned)k * GS + FRG);
      spin_flag(fb + F_CNT + 4 * 8, ALL4);
      static_for<WLA>([&](auto I) { issue_reads(std::integral_constant<int, 64 + (int)I>{}); });
    }
    if constexpr (n >= 64) static_for<VS.start[n + 1] - VS.start[n]>([&](auto K) { vop(std::integral_constant<int, VS.start[n] + (int)K>{}); });
    if constexpr (n == 188) {
      lw = swapadd(lsum);
      f32x2 st = {mref, lw};
      *(LAS f32x2*)(uintptr_t)(lin + (unsigned)(1 + wid) * GS + MISC) = st;
    }
    if constexpr (n == 189) {
      if (__builtin_expect((unsigned)__builtin_amdgcn_readfirstlane(relq) != ALL4, 0)) spin_flag(fb + F_REL + 4 * PL.erel, ALL4);
      cfence();
      osend(0);
    }
    if constexpr (n == 190) { osend(1); lds_set(fb + F_O1 + 4 * wid); }
    SB();
  });
  STAMP(30);
  CST(24);
  spin_flag(fb + F_O1 + 4 * (wid ^ 2), 1u);
  STAMP(31);
  const int lane = fresh_lane();
  const __amdgpu_buffer_rsrc_t wsr = __builtin_amdgcn_make_buffer_rsrc(ws, 0, 16u << 20, 0x00020000);
  bf16x8 of[4][4];
  {
    const f32x2 so2 = *(const LAS f32x2*)(uintptr_t)(lin + (unsigned)(1 + (wid ^ 2)) * GS + MISC);
    u32x4 v[4];
#pragma unroll
    for (int k = 0; k < 4; ++k) v[k] = *(const LAS u32x4*)(uintptr_t)(lin + oth4 + exc(k));
    const float M = fmaxf(mref, so2[0]);
    const float bs = __builtin_amdgcn_exp2f(mref - M), bo = __builtin_amdgcn_exp2f(so2[0] - M);
    const float inv = 1.0f / (lw * bs + so2[1] * bo);
    const float fs = bs * inv, fo2 = bo * inv;
    const unsigned goff = WS_O + (unsigned)(((bid * 2 + r) * 4 + 2 * c) * 2) * 1024 + lane * 16;
#pragma unroll
    for (int i = 0; i < 2; ++i) {
      f32x16 t;
#pragma unroll
      for (int e = 0; e < 16; ++e) {
        const unsigned w = v[i * 2 + (e >> 3)][(e & 7) >> 1];
        t[e] = fmaf(__uint_as_float((e & 1) ? (w & 0xffff0000u) : (w << 16)), fo2, oacc[i][e] * fs);
      }
#pragma unroll
      for (int s = 0; s < 2; ++s) {
        of[0][i * 2 + s] = pack8(t, s);
        __builtin_amdgcn_raw_buffer_store_b128(__builtin_bit_cast(u32x4, of[0][i * 2 + s]), wsr, goff + (i * 2 + s) * 1024, 0, 16);
      }
    }
    cfence();
  }
  unsigned ob[4][2], oa[2][4][2][2];
#pragma unroll
  for (int k = 0; k < 4; ++k)
#pragma unroll
    for (int tt = 0; tt < 2; ++tt) ob[k][tt] = ldsb + trV(lane, k ^ (2 * c), tt);
  auto oaddr = [&](auto JJ) {
    constexpr int jj = JJ;
    const unsigned so = c ? soff(28 + 2 * jj + 1) : soff(28 + 2 * jj);
#pragma unroll
    for (int k = 0; k < 4; ++k)
#pragma unroll
      for (int tt = 0; tt < 2; ++tt) { oa[jj & 1][k][tt][0] = opaque(ob[k][tt] + so); oa[jj & 1][k][tt][1] = opaque(ob[k][tt] + so + 8u * GS); }
  };
  bf16x8 fo[64];
  f32x16 pacc[4] = {};
  auto oreads = [&](auto M) {
    constexpr int m = M, ks4 = op_ks(m), k = op_ct(m), jj = m / 16;
    constexpr unsigned so = (4 * (ks4 & 1)) * GS;
    if constexpr (m % 16 == 0) { spin_flag(fb + F_WO + 4 * jj, ALL4); oaddr(std::integral_constant<int, jj>{}); }
    fo[m] = trread2(oa[jj & 1][k][0][ks4 >> 1] + so, oa[jj & 1][k][1][ks4 >> 1] + so);
    if constexpr (m == 15) lds_set(fbw + F_O2);
  };
  STAMP(32);
  asm volatile("s_waitcnt vmcnt(0)" ::: "memory");
  if (lane == 0) __hip_atomic_store((gu32*)(ws + WS_F1) + (b * 8 + g) * FSTR + hh * 4 + wid, tag, RLX_AGENT);
  STAMP(33);
  static_for<OLA0>([&](auto M) { oreads(M); });
  STAMP(34);
  {
    gu32* f1 = (gu32*)(ws + WS_F1) + (b * 8 + g) * FSTR + ((hh + 1 + min(lane, 2)) & 3) * 4 + wid;
    for (unsigned spins = 0; spins < (1u << 17); ++spins) {
      if (__all(__hip_atomic_load(f1, RLX_AGENT) == tag)) break;
    }
    asm volatile("" ::: "memory");
    STAMP(35);
#pragma unroll
    for (int j2 = 1; j2 < 4; ++j2) {
      const int sb = (bid & ~24) | (((hh + j2) & 3) << 3);
#pragma unroll
      for (int k = 0; k < 4; ++k)
        of[j2][k] = __builtin_bit_cast(bf16x8, __builtin_amdgcn_raw_buffer_load_b128(wsr, WS_O + (unsigned)((sb * 2 + r) * 8 + 4 * c + k) * 1024 + lane * 16, 0, 16));
    }
  }
  const unsigned xo = lin + (unsigned)r * (8 * GS) + (c ? soff(29) : soff(28)), xi = lin + (unsigned)r * (8 * GS) + (c ? soff(28) : soff(29));
  const unsigned xo0 = opaque(xo), xo1 = opaque(xo + 4 * GS), xi0 = opaque(xi), xi1 = opaque(xi + 4 * GS);
  unsigned relo = 0;
  SB();
  static_for<64>([&](auto M) {
    constexpr int m = M, jj = m / 16;
    pacc[op_ct(m)] = MFMA32(fo[m], of[jj][op_ks(m)], pacc[op_ct(m)]);
    SB();
    static_for<2>([&](auto I) {
      constexpr int mm = op_first(m) + (int)I;
      if constexpr (mm < 64) { if constexpr (op_isl(mm) == m) oreads(std::integral_constant<int, mm>{}); }
    });
    if constexpr (m == 50) relo = ldsflag(fb + F_O2);
    if constexpr (m == 57) {
      if (__builtin_expect((unsigned)__builtin_amdgcn_readfirstlane(relo) != ALL4, 0)) spin_flag(fb + F_O2, ALL4);
      cfence();
#pragma unroll
      for (int gq = 0; gq < 4; ++gq) { f32x4 v = {pacc[2][4 * gq], pacc[2][4 * gq + 1], pacc[2][4 * gq + 2], pacc[2][4 * gq + 3]}; *(LAS f32x4*)(uintptr_t)(xo0 + (unsigned)gq * GS) = v; }
    }
    if constexpr (m == 58) {
#pragma unroll
      for (int gq = 0; gq < 4; ++gq) { f32x4 v = {pacc[3][4 * gq], pacc[3][4 * gq + 1], pacc[3][4 * gq + 2], pacc[3][4 * gq + 3]}; *(LAS f32x4*)(uintptr_t)(xo1 + (unsigned)gq * GS) = v; }
      lds_set(fb + F_P + 4 * wid);
    }
    SB();
  });
  CST(25);
  STAMP(36);
  spin_flag(fb + F_P + 4 * (wid ^ 2), 1u);
  {
    f32x4 v[8];
#pragma unroll
    for (int k = 0; k < 8; ++k) v[k] = *(const LAS f32x4*)(uintptr_t)((k < 4 ? xi0 : xi1) + (unsigned)(k & 3) * GS);
#pragma unroll
    for (int ci = 0; ci < 2; ++ci) {
      const int cons = (bid & ~7) | (2 * (2 * c + ci) + r);
      f32x16 t;
#pragma unroll
      for (int e = 0; e < 16; ++e) t[e] = pacc[ci][e] + v[ci * 4 + (e >> 2)][e & 3];
#pragma unroll
      for (int s = 0; s < 2; ++s)
        __builtin_amdgcn_raw_buffer_store_b128(__builtin_bit_cast(u32x4, pack8(t, s)), wsr, WS_P + (unsigned)((cons * 8 + g) * 2 + s) * 1024 + lane * 16, 0, 16);
    }
  }
  asm volatile("s_waitcnt vmcnt(0)" ::: "memory");
  if (lane < 2) {
    const int cons = (bid & ~7) | (2 * (2 * c + lane) + r);
    __hip_atomic_store((gu32*)(ws + WS_F2) + cons * FSTR + g, tag, RLX_AGENT);
  }
  STAMP(37);
  {
    gu32* f2 = (gu32*)(ws + WS_F2) + bid * FSTR + (lane & 7);
    const unsigned poff = WS_P + (unsigned)((bid * 8 + (lane >> 5) * 4) * 2 + (wid & 1)) * 1024 + ((wid >> 1) * 32 + (lane & 31)) * 16;
    for (unsigned spins = 0; spins < (1u << 17); ++spins) {
      if (__all(__hip_atomic_load(f2, RLX_AGENT) == tag)) break;
    }
    asm volatile("" ::: "memory");
    STAMP(38);
    if ((bid & 31) == 0 && wid == 0 && lane == 0) __hip_atomic_store((gu32*)(ws + WS_EP) + b * 32, tag, RLX_AGENT);
    u32x4 pp[4];
#pragma unroll
    for (int p = 0; p < 4; ++p) pp[p] = __builtin_amdgcn_raw_buffer_load_b128(wsr, poff + p * 2048, 0, 16);
    float sm[8];
#pragma unroll
    for (int e = 0; e < 8; ++e) {
      float q4[4];
#pragma unroll
      for (int p = 0; p < 4; ++p) { const unsigned w = pp[p][e >> 1]; q4[p] = __uint_as_float((e & 1) ? (w & 0xffff0000u) : (w << 16)); }
      sm[e] = (q4[0] + q4[1]) + (q4[2] + q4[3]);
    }
    float s[4];
#pragma unroll
    for (int e = 0; e < 4; ++e) {
      auto rr = __builtin_amdgcn_permlane32_swap(__float_as_uint(sm[e]), __float_as_uint(sm[4 + e]), false, false);
      s[e] = __uint_as_float(rr[0]) + __uint_as_float(rr[1]);
    }
    const int ct = g >> 1, rr = g & 1;
    u32x2 o2 = {pk2(s[0], s[1]), pk2(s[2], s[3])};
    *(u32x2*)(out + (size_t)(b * 64 + rr * 32 + (lane & 31)) * 512 + hh * 128 + ct * 32 + 8 * (2 * (wid & 1) + (lane >> 5)) + 4 * (wid >> 1)) = o2;
  }
  STAMP(39);
}

extern "C" void kernel_launch(void* const* d_in, const int* in_sizes, int n_in, void* d_out, int out_size, void* d_ws, size_t ws_size, hipStream_t stream) {
  (void)hipFuncSetAttribute((const void*)gqa_kernel, hipFuncAttributeMaxDynamicSharedMemorySize, LDSB);
  gqa_kernel<<<256, 512, LDSB, stream>>>((const u16*)d_in[0], (const u16*)d_in[1], (const u16*)d_in[2], (const u16*)d_in[3], (const u16*)d_in[4], (char*)d_ws, (u16*)d_out);
}
```
